```python
import torch

def _softmax(x, axis=-1):
    return torch.softmax(x, dim=axis)
def _gelu(x):
    return torch.nn.functional.gelu(x, approximate="tanh")
def _silu(x):
    return torch.nn.functional.silu(x)
def _sigmoid(x):
    return torch.sigmoid(x)
def _layernorm(x, axis=-1, eps=1e-5):
    m = x.mean(dim=axis, keepdim=True)
    v = x.var(dim=axis, keepdim=True, unbiased=False)
    return (x - m) / torch.sqrt(v + eps)
def _scatter_add(tgt, idx, src, axis=0):
    return tgt.clone().index_add_(axis, idx, src)
def _segment_sum(src, seg, n):
    out = torch.zeros((n,) + src.shape[1:], dtype=src.dtype)
    return out.index_add_(0, seg, src)
def _pad(x, pw):
    flat = tuple(v for pair in reversed(pw) for v in pair)
    return torch.nn.functional.pad(x, flat)

kernel_name = 'standalone__rmsnorm__sampling'

def setup_inputs():

    _w = (lambda a: (lambda t: t.to(torch.bfloat16) if t.is_floating_point() else t)(torch.as_tensor(a)))
    B, S, D = 1024, 16, 448
    return {
        "x": _w((lambda *s: torch.randn(*s, dtype=torch.bfloat16) * 0.05)(B, S, D)),
        "g": _w((lambda *s: torch.randn(*s, dtype=torch.bfloat16) * 0.05)(D) + 1.0),
    }

def reference(x, g):
    rms = torch.sqrt((x * x).mean(axis=-1, keepdims=True) + 1e-6)
    return x / rms * g
```

```cpp
#include <hip/hip_runtime.h>
#include <hip/hip_bf16.h>
#include <stdint.h>

typedef unsigned int u32x4 __attribute__((ext_vector_type(4)));
typedef float f32x2 __attribute__((ext_vector_type(2)));
typedef __bf16 bf16x2 __attribute__((ext_vector_type(2)));

constexpr int D = 448;
constexpr int CH = 7;
constexpr int TPB = 256;

__device__ __forceinline__ float bf_lo(unsigned int u) { return __uint_as_float(u << 16); }
__device__ __forceinline__ float bf_hi(unsigned int u) { return __uint_as_float(u & 0xffff0000u); }
__device__ __forceinline__ unsigned int pack_bf16(float lo, float hi) {
    f32x2 f = {lo, hi};
    bf16x2 b = __builtin_convertvector(f, bf16x2);
    return __builtin_bit_cast(unsigned int, b);
}

template <int CTRL>
__device__ __forceinline__ float dpp_add(float v) {
    int t = __builtin_amdgcn_update_dpp(0, __float_as_int(v), CTRL, 0xf, 0xf, true);
    return v + __int_as_float(t);
}

__global__ __launch_bounds__(TPB) void rmsnorm448_kernel(const u32x4* __restrict__ x,
                                                         const u32x4* __restrict__ g,
                                                         u32x4* __restrict__ out, int rows) {
    const int tid = blockIdx.x * TPB + threadIdx.x;
    const int row = tid >> 3;
    const int sub = tid & 7;
    if (row >= rows) return;
    const u32x4* xr = x + (size_t)row * 56 + sub;
    u32x4* orow = out + (size_t)row * 56 + sub;
    u32x4 v[CH], gv[CH];
#pragma unroll
    for (int k = 0; k < CH; ++k) v[k] = xr[k * 8];
#pragma unroll
    for (int k = 0; k < CH; ++k) gv[k] = g[k * 8 + sub];
    float ss = 0.f;
#pragma unroll
    for (int k = 0; k < CH; ++k) {
#pragma unroll
        for (int j = 0; j < 4; ++j) {
            float a = bf_lo(v[k][j]), b = bf_hi(v[k][j]);
            ss = fmaf(a, a, ss);
            ss = fmaf(b, b, ss);
        }
    }
    ss = dpp_add<0xB1>(ss);
    ss = dpp_add<0x4E>(ss);
    ss = dpp_add<0x141>(ss);
    const float inv = rsqrtf(ss * (1.0f / D) + 1e-6f);
#pragma unroll
    for (int k = 0; k < CH; ++k) {
        u32x4 o;
#pragma unroll
        for (int j = 0; j < 4; ++j) {
            float a = bf_lo(v[k][j]) * inv * bf_lo(gv[k][j]);
            float b = bf_hi(v[k][j]) * inv * bf_hi(gv[k][j]);
            o[j] = pack_bf16(a, b);
        }
        orow[k * 8] = o;
    }
}

extern "C" void kernel_launch(void* const* d_in, const int* in_sizes, int n_in,
                              void* d_out, int out_size, void* d_ws, size_t ws_size,
                              hipStream_t stream) {
    const u32x4* x = (const u32x4*)d_in[0];
    const u32x4* g = (const u32x4*)d_in[1];
    u32x4* out = (u32x4*)d_out;
    int rows = in_sizes[0] / D;
    int threads = rows * 8;
    int grid = (threads + TPB - 1) / TPB;
    rmsnorm448_kernel<<<grid, TPB, 0, stream>>>(x, g, out, rows);
}
```

```cpp
#include <hip/hip_runtime.h>
#include <hip/hip_bf16.h>
#include <stdint.h>

#define LD_AUX @LD_AUX@
#define ST_AUX @ST_AUX@
#define TPB @TPB@

typedef unsigned int u32x4 __attribute__((ext_vector_type(4)));
typedef float f32x2 __attribute__((ext_vector_type(2)));
typedef __bf16 bf16x2 __attribute__((ext_vector_type(2)));

constexpr int D = 448;
constexpr int CH = 7;

__device__ __forceinline__ float bf_lo(unsigned int u) { return __uint_as_float(u << 16); }
__device__ __forceinline__ float bf_hi(unsigned int u) { return __uint_as_float(u & 0xffff0000u); }
__device__ __forceinline__ unsigned int pack_bf16(float lo, float hi) {
    f32x2 f = {lo, hi};
    bf16x2 b = __builtin_convertvector(f, bf16x2);
    return __builtin_bit_cast(unsigned int, b);
}

template <int CTRL>
__device__ __forceinline__ float dpp_add(float v) {
    int t = __builtin_amdgcn_update_dpp(0, __float_as_int(v), CTRL, 0xf, 0xf, true);
    return v + __int_as_float(t);
}

__global__ __launch_bounds__(TPB) void rmsnorm448_kernel(const void* __restrict__ x,
                                                         const void* __restrict__ g,
                                                         void* __restrict__ out, int nbytes) {
    const int tid = blockIdx.x * TPB + threadIdx.x;
    const int row = tid >> 3;
    const int sub = tid & 7;
    auto xs = __builtin_amdgcn_make_buffer_rsrc((void*)x, 0, nbytes, 0x00020000);
    auto gs = __builtin_amdgcn_make_buffer_rsrc((void*)g, 0, D * 2, 0x00020000);
    auto os = __builtin_amdgcn_make_buffer_rsrc(out, 0, nbytes, 0x00020000);
    const int off = row * (D * 2) + sub * 16;
    u32x4 v[CH], gv[CH];
#pragma unroll
    for (int k = 0; k < CH; ++k) v[k] = __builtin_amdgcn_raw_buffer_load_b128(xs, off + k * 128, 0, LD_AUX);
#pragma unroll
    for (int k = 0; k < CH; ++k) gv[k] = __builtin_amdgcn_raw_buffer_load_b128(gs, sub * 16 + k * 128, 0, 0);
    float ss0 = 0.f, ss1 = 0.f;
#pragma unroll
    for (int k = 0; k < CH; ++k) {
#pragma unroll
        for (int j = 0; j < 4; ++j) {
            float a = bf_lo(v[k][j]), b = bf_hi(v[k][j]);
            ss0 = fmaf(a, a, ss0);
            ss1 = fmaf(b, b, ss1);
        }
    }
    float ss = ss0 + ss1;
    ss = dpp_add<0xB1>(ss);
    ss = dpp_add<0x4E>(ss);
    ss = dpp_add<0x141>(ss);
    const float inv = __builtin_amdgcn_rsqf(ss * (1.0f / D) + 1e-6f);
#pragma unroll
    for (int k = 0; k < CH; ++k) {
        u32x4 o;
#pragma unroll
        for (int j = 0; j < 4; ++j) {
            float a = bf_lo(v[k][j]) * inv * bf_lo(gv[k][j]);
            float b = bf_hi(v[k][j]) * inv * bf_hi(gv[k][j]);
            o[j] = pack_bf16(a, b);
        }
        __builtin_amdgcn_raw_buffer_store_b128(o, os, off + k * 128, 0, ST_AUX);
    }
}

extern "C" void kernel_launch(void* const* d_in, const int* in_sizes, int n_in,
                              void* d_out, int out_size, void* d_ws, size_t ws_size,
                              hipStream_t stream) {
    int rows = in_sizes[0] / D;
    int threads = rows * 8;
    int grid = (threads + TPB - 1) / TPB;
    rmsnorm448_kernel<<<grid, TPB, 0, stream>>>(d_in[0], d_in[1], d_out, rows * D * 2);
}
```

```cpp
#include <hip/hip_runtime.h>
#include <hip/hip_bf16.h>
#include <stdint.h>

#define LD_AUX @LD_AUX@
#define ST_AUX @ST_AUX@
#define TPB @TPB@
#define REPEAT @REPEAT@
#define SWZ @SWZ@

typedef unsigned int u32x4 __attribute__((ext_vector_type(4)));
typedef float f32x2 __attribute__((ext_vector_type(2)));
typedef __bf16 bf16x2 __attribute__((ext_vector_type(2)));

constexpr int D = 448;
constexpr int CH = 7;

__device__ __forceinline__ float bf_lo(unsigned int u) { return __uint_as_float(u << 16); }
__device__ __forceinline__ float bf_hi(unsigned int u) { return __uint_as_float(u & 0xffff0000u); }
__device__ __forceinline__ unsigned int pack_bf16(float lo, float hi) {
    f32x2 f = {lo, hi};
    bf16x2 b = __builtin_convertvector(f, bf16x2);
    return __builtin_bit_cast(unsigned int, b);
}

template <int CTRL>
__device__ __forceinline__ float dpp_add(float v) {
    int t = __builtin_amdgcn_update_dpp(0, __float_as_int(v), CTRL, 0xf, 0xf, true);
    return v + __int_as_float(t);
}

__global__ __launch_bounds__(TPB) void rmsnorm448_kernel(const void* __restrict__ x,
                                                         const void* __restrict__ g,
                                                         void* __restrict__ out, int nbytes) {
    #if SWZ
    const int bid = (blockIdx.x & 7) * (gridDim.x >> 3) + (blockIdx.x >> 3);
#else
    const int bid = blockIdx.x;
#endif
    const int tid = bid * TPB + threadIdx.x;
    const int row = tid >> 3;
    const int sub = tid & 7;
    auto xs = __builtin_amdgcn_make_buffer_rsrc((void*)x, 0, nbytes, 0x00020000);
    auto gs = __builtin_amdgcn_make_buffer_rsrc((void*)g, 0, D * 2, 0x00020000);
    auto os = __builtin_amdgcn_make_buffer_rsrc(out, 0, nbytes, 0x00020000);
    const int off = row * (D * 2) + sub * 16;
    u32x4 v[CH], gv[CH];
#pragma unroll
    for (int k = 0; k < CH; ++k) v[k] = __builtin_amdgcn_raw_buffer_load_b128(xs, off + k * 128, 0, LD_AUX);
#pragma unroll
    for (int k = 0; k < CH; ++k) gv[k] = __builtin_amdgcn_raw_buffer_load_b128(gs, sub * 16 + k * 128, 0, 0);
    float ss0 = 0.f, ss1 = 0.f;
#pragma unroll
    for (int k = 0; k < CH; ++k) {
#pragma unroll
        for (int j = 0; j < 4; ++j) {
            float a = bf_lo(v[k][j]), b = bf_hi(v[k][j]);
            ss0 = fmaf(a, a, ss0);
            ss1 = fmaf(b, b, ss1);
        }
    }
    float ss = ss0 + ss1;
    ss = dpp_add<0xB1>(ss);
    ss = dpp_add<0x4E>(ss);
    ss = dpp_add<0x141>(ss);
    const float inv = __builtin_amdgcn_rsqf(ss * (1.0f / D) + 1e-6f);
#pragma unroll
    for (int k = 0; k < CH; ++k) {
        u32x4 o;
#pragma unroll
        for (int j = 0; j < 4; ++j) {
            float a = bf_lo(v[k][j]) * inv * bf_lo(gv[k][j]);
            float b = bf_hi(v[k][j]) * inv * bf_hi(gv[k][j]);
            o[j] = pack_bf16(a, b);
        }
        __builtin_amdgcn_raw_buffer_store_b128(o, os, off + k * 128, 0, ST_AUX);
    }
}

extern "C" void kernel_launch(void* const* d_in, const int* in_sizes, int n_in,
                              void* d_out, int out_size, void* d_ws, size_t ws_size,
                              hipStream_t stream) {
    int rows = in_sizes[0] / D;
    int threads = rows * 8;
    int grid = (threads + TPB - 1) / TPB;
    for (int r = 0; r < REPEAT; ++r)
    rmsnorm448_kernel<<<grid, TPB, 0, stream>>>(d_in[0], d_in[1], d_out, rows * D * 2);
}
```

```cpp
#include <hip/hip_runtime.h>
#include <hip/hip_bf16.h>
#include <stdint.h>

#define LD_AUX @LD_AUX@
#define ST_AUX @ST_AUX@
#define TPB 256
#define QSEL @QSEL@
#define KMUL @KMUL@

typedef unsigned int u32x4 __attribute__((ext_vector_type(4)));
typedef float f32x2 __attribute__((ext_vector_type(2)));
typedef __bf16 bf16x2 __attribute__((ext_vector_type(2)));
constexpr int D = 448;
constexpr int CH = 7;
__device__ __forceinline__ float bf_lo(unsigned int u) { return __uint_as_float(u << 16); }
__device__ __forceinline__ float bf_hi(unsigned int u) { return __uint_as_float(u & 0xffff0000u); }
__device__ __forceinline__ unsigned int pack_bf16(float lo, float hi) {
    f32x2 f = {lo, hi};
    bf16x2 b = __builtin_convertvector(f, bf16x2);
    return __builtin_bit_cast(unsigned int, b);
}
template <int CTRL>
__device__ __forceinline__ float dpp_add(float v) {
    int t = __builtin_amdgcn_update_dpp(0, __float_as_int(v), CTRL, 0xf, 0xf, true);
    return v + __int_as_float(t);
}
__device__ __forceinline__ unsigned long long stamp() {
    unsigned long long t;
    __builtin_amdgcn_sched_barrier(0);
    asm volatile("s_memrealtime %0\n\ts_waitcnt lgkmcnt(0)" : "=s"(t) :: "memory");
    __builtin_amdgcn_sched_barrier(0);
    return t;
}
constexpr int NS = 8;
__global__ __launch_bounds__(TPB) void tl_kernel(const void* __restrict__ x, const void* __restrict__ g,
                                                 void* __restrict__ out, int nbytes, unsigned long long* __restrict__ dbg) {
    unsigned long long t0 = stamp();
    const int tid = blockIdx.x * TPB + threadIdx.x;
    const int row = tid >> 3;
    const int sub = tid & 7;
    auto xs = __builtin_amdgcn_make_buffer_rsrc((void*)x, 0, nbytes, 0x00020000);
    auto gs = __builtin_amdgcn_make_buffer_rsrc((void*)g, 0, D * 2, 0x00020000);
    auto os = __builtin_amdgcn_make_buffer_rsrc(out, 0, nbytes, 0x00020000);
    const int off = row * (D * 2) + sub * 16;
    asm volatile("" :: "s"(x), "s"(out), "s"(g));
    unsigned long long t1 = stamp();
    u32x4 v[CH], gv[CH];
#pragma unroll
    for (int k = 0; k < CH; ++k) v[k] = __builtin_amdgcn_raw_buffer_load_b128(xs, off + k * 128, 0, LD_AUX);
#pragma unroll
    for (int k = 0; k < CH; ++k) gv[k] = __builtin_amdgcn_raw_buffer_load_b128(gs, sub * 16 + k * 128, 0, 0);
    unsigned long long t2 = stamp();
    asm volatile("s_waitcnt vmcnt(13)" : "+v"(v[0]) :: "memory");
    unsigned long long t3 = stamp();
    asm volatile("s_waitcnt vmcnt(0)" : "+v"(v[0]), "+v"(v[1]), "+v"(v[2]), "+v"(v[3]), "+v"(v[4]), "+v"(v[5]), "+v"(v[6]) :: "memory");
    unsigned long long t4 = stamp();
    float ss0 = 0.f, ss1 = 0.f;
#pragma unroll
    for (int k = 0; k < CH; ++k) {
#pragma unroll
        for (int j = 0; j < 4; ++j) {
            float a = bf_lo(v[k][j]), b = bf_hi(v[k][j]);
            ss0 = fmaf(a, a, ss0);
            ss1 = fmaf(b, b, ss1);
        }
    }
    float ss = ss0 + ss1;
    ss = dpp_add<0xB1>(ss);
    ss = dpp_add<0x4E>(ss);
    ss = dpp_add<0x141>(ss);
    const float inv = __builtin_amdgcn_rsqf(ss * (1.0f / D) + 1e-6f);
#pragma unroll
    for (int k = 0; k < CH; ++k) {
        u32x4 o;
#pragma unroll
        for (int j = 0; j < 4; ++j) {
            float a = bf_lo(v[k][j]) * inv * bf_lo(gv[k][j]);
            float b = bf_hi(v[k][j]) * inv * bf_hi(gv[k][j]);
            o[j] = pack_bf16(a, b);
        }
        __builtin_amdgcn_raw_buffer_store_b128(o, os, off + k * 128, 0, ST_AUX);
    }
    unsigned long long t5 = stamp();
    asm volatile("s_waitcnt vmcnt(0)" ::: "memory");
    unsigned long long t6 = stamp();
    if ((threadIdx.x & 63) == 0) {
        unsigned long long* p = dbg + (size_t)(tid >> 6) * NS;
        p[0] = t0; p[1] = t1; p[2] = t2; p[3] = t3; p[4] = t4; p[5] = t5; p[6] = t6; p[7] = 0;
    }
}

__global__ __launch_bounds__(256) void spin_kernel(const unsigned long long* __restrict__ dbg, int nw) {
    unsigned long long tent = __builtin_amdgcn_s_memrealtime();
    __shared__ unsigned long long smin[256], smax[256], ssum[256];
    unsigned long long mn = ~0ull, mx = 0, sm = 0;
    const int s = QSEL == 0 ? 0 : (QSEL <= 7 ? QSEL - 1 : (QSEL <= 17 ? QSEL - 11 : 6));
    for (int w = threadIdx.x; w < nw; w += 256) {
        unsigned long long a = dbg[w * NS], b = dbg[w * NS + s];
        mn = a < mn ? a : mn; mx = b > mx ? b : mx; sm += (b & 0xffffffffffull);
    }
    smin[threadIdx.x] = mn; smax[threadIdx.x] = mx; ssum[threadIdx.x] = sm;
    __syncthreads();
    if (threadIdx.x == 0) {
        for (int i = 1; i < 256; ++i) { mn = smin[i] < mn ? smin[i] : mn; mx = smax[i] > mx ? smax[i] : mx; sm += ssum[i]; }
        unsigned long long q = 0;
        if (QSEL >= 1 && QSEL <= 7) q = mx - mn;
        else if (QSEL >= 11 && QSEL <= 17) q = sm / nw - (mn & 0xffffffffffull);
        else if (QSEL == 20) q = tent - mx;
        else if (QSEL == 21) q = __builtin_amdgcn_s_memrealtime() - mn;
        else if (QSEL == 22) q = __builtin_amdgcn_s_memrealtime() - tent;
        if (q > 3000) q = 3000;
        unsigned long long now = __builtin_amdgcn_s_memrealtime();
        unsigned long long until = now + q * KMUL;
        while (__builtin_amdgcn_s_memrealtime() < until) __builtin_amdgcn_s_sleep(1);
    }
}

extern "C" void kernel_launch(void* const* d_in, const int* in_sizes, int n_in,
                              void* d_out, int out_size, void* d_ws, size_t ws_size,
                              hipStream_t stream) {
    int rows = in_sizes[0] / D;
    int threads = rows * 8;
    int grid = (threads + TPB - 1) / TPB;
    tl_kernel<<<grid, TPB, 0, stream>>>(d_in[0], d_in[1], d_out, rows * D * 2, (unsigned long long*)d_ws);
    spin_kernel<<<1, 256, 0, stream>>>((const unsigned long long*)d_ws, rows / 8);
}
```

```cpp
#include <hip/hip_runtime.h>
#include <hip/hip_bf16.h>
#include <stdint.h>

#define LD_AUX @LD_AUX@
#define ST_AUX @ST_AUX@
#define TPB @TPB@
#define REPEAT @REPEAT@
#define SWZ @SWZ@
#define TAILSPIN @TAILSPIN@

typedef unsigned int u32x4 __attribute__((ext_vector_type(4)));
typedef float f32x2 __attribute__((ext_vector_type(2)));
typedef __bf16 bf16x2 __attribute__((ext_vector_type(2)));

constexpr int D = 448;
constexpr int CH = 7;

__device__ __forceinline__ float bf_lo(unsigned int u) { return __uint_as_float(u << 16); }
__device__ __forceinline__ float bf_hi(unsigned int u) { return __uint_as_float(u & 0xffff0000u); }
__device__ __forceinline__ unsigned int pack_bf16(float lo, float hi) {
    f32x2 f = {lo, hi};
    bf16x2 b = __builtin_convertvector(f, bf16x2);
    return __builtin_bit_cast(unsigned int, b);
}

template <int CTRL>
__device__ __forceinline__ float dpp_add(float v) {
    int t = __builtin_amdgcn_update_dpp(0, __float_as_int(v), CTRL, 0xf, 0xf, true);
    return v + __int_as_float(t);
}

__global__ __launch_bounds__(TPB) void rmsnorm448_kernel(const void* __restrict__ x,
                                                         const void* __restrict__ g,
                                                         void* __restrict__ out, int nbytes) {
    #if SWZ
    const int bid = (blockIdx.x & 7) * (gridDim.x >> 3) + (blockIdx.x >> 3);
#else
    const int bid = blockIdx.x;
#endif
    const int tid = bid * TPB + threadIdx.x;
    const int row = tid >> 3;
    const int sub = tid & 7;
    auto xs = __builtin_amdgcn_make_buffer_rsrc((void*)x, 0, nbytes, 0x00020000);
    auto gs = __builtin_amdgcn_make_buffer_rsrc((void*)g, 0, D * 2, 0x00020000);
    auto os = __builtin_amdgcn_make_buffer_rsrc(out, 0, nbytes, 0x00020000);
    const int off = row * (D * 2) + sub * 16;
    u32x4 v[CH], gv[CH];
#pragma unroll
    for (int k = 0; k < CH; ++k) v[k] = __builtin_amdgcn_raw_buffer_load_b128(xs, off + k * 128, 0, LD_AUX);
#pragma unroll
    for (int k = 0; k < CH; ++k) gv[k] = __builtin_amdgcn_raw_buffer_load_b128(gs, sub * 16 + k * 128, 0, 0);
    float ss0 = 0.f, ss1 = 0.f;
#pragma unroll
    for (int k = 0; k < CH; ++k) {
#pragma unroll
        for (int j = 0; j < 4; ++j) {
            float a = bf_lo(v[k][j]), b = bf_hi(v[k][j]);
            ss0 = fmaf(a, a, ss0);
            ss1 = fmaf(b, b, ss1);
        }
    }
    float ss = ss0 + ss1;
    ss = dpp_add<0xB1>(ss);
    ss = dpp_add<0x4E>(ss);
    ss = dpp_add<0x141>(ss);
    const float inv = __builtin_amdgcn_rsqf(ss * (1.0f / D) + 1e-6f);
#pragma unroll
    for (int k = 0; k < CH; ++k) {
        u32x4 o;
#pragma unroll
        for (int j = 0; j < 4; ++j) {
            float a = bf_lo(v[k][j]) * inv * bf_lo(gv[k][j]);
            float b = bf_hi(v[k][j]) * inv * bf_hi(gv[k][j]);
            o[j] = pack_bf16(a, b);
        }
        __builtin_amdgcn_raw_buffer_store_b128(o, os, off + k * 128, 0, ST_AUX);
    }
}

__global__ void tail_kernel(int ticks) {
    if (ticks > 0) {
        unsigned long long until = __builtin_amdgcn_s_memrealtime() + ticks;
        while (__builtin_amdgcn_s_memrealtime() < until) __builtin_amdgcn_s_sleep(1);
    }
}

extern "C" void kernel_launch(void* const* d_in, const int* in_sizes, int n_in,
                              void* d_out, int out_size, void* d_ws, size_t ws_size,
                              hipStream_t stream) {
    int rows = in_sizes[0] / D;
    int threads = rows * 8;
    int grid = (threads + TPB - 1) / TPB;
    for (int r = 0; r < REPEAT; ++r)
    rmsnorm448_kernel<<<grid, TPB, 0, stream>>>(d_in[0], d_in[1], d_out, rows * D * 2);
    if (TAILSPIN >= 0) tail_kernel<<<1, 64, 0, stream>>>(TAILSPIN);
}
```

```cpp
#include <hip/hip_runtime.h>
#include <hip/hip_bf16.h>
#include <stdint.h>

#define LD_AUX @LD_AUX@
#define ST_AUX @ST_AUX@
#define TPB 256
#define QSEL @QSEL@
#define KMUL @KMUL@

typedef unsigned int u32x4 __attribute__((ext_vector_type(4)));
typedef float f32x2 __attribute__((ext_vector_type(2)));
typedef __bf16 bf16x2 __attribute__((ext_vector_type(2)));
constexpr int D = 448;
constexpr int CH = 7;
__device__ __forceinline__ float bf_lo(unsigned int u) { return __uint_as_float(u << 16); }
__device__ __forceinline__ float bf_hi(unsigned int u) { return __uint_as_float(u & 0xffff0000u); }
__device__ __forceinline__ unsigned int pack_bf16(float lo, float hi) {
    f32x2 f = {lo, hi};
    bf16x2 b = __builtin_convertvector(f, bf16x2);
    return __builtin_bit_cast(unsigned int, b);
}
template <int CTRL>
__device__ __forceinline__ float dpp_add(float v) {
    int t = __builtin_amdgcn_update_dpp(0, __float_as_int(v), CTRL, 0xf, 0xf, true);
    return v + __int_as_float(t);
}
__device__ __forceinline__ unsigned long long stamp() {
    unsigned long long t;
    __builtin_amdgcn_sched_barrier(0);
    asm volatile("s_memrealtime %0\n\ts_waitcnt lgkmcnt(0)" : "=s"(t) :: "memory");
    __builtin_amdgcn_sched_barrier(0);
    return t;
}
constexpr int NS = 8;
__global__ __launch_bounds__(TPB) void tl_kernel(const void* __restrict__ x, const void* __restrict__ g,
                                                 void* __restrict__ out, int nbytes, unsigned long long* __restrict__ dbg) {
    unsigned long long t0 = stamp();
    const int tid = blockIdx.x * TPB + threadIdx.x;
    const int row = tid >> 3;
    const int sub = tid & 7;
    auto xs = __builtin_amdgcn_make_buffer_rsrc((void*)x, 0, nbytes, 0x00020000);
    auto gs = __builtin_amdgcn_make_buffer_rsrc((void*)g, 0, D * 2, 0x00020000);
    auto os = __builtin_amdgcn_make_buffer_rsrc(out, 0, nbytes, 0x00020000);
    const int off = row * (D * 2) + sub * 16;
    asm volatile("" :: "s"(x), "s"(out), "s"(g));
    unsigned long long t1 = stamp();
    u32x4 v[CH], gv[CH];
#pragma unroll
    for (int k = 0; k < CH; ++k) v[k] = __builtin_amdgcn_raw_buffer_load_b128(xs, off + k * 128, 0, LD_AUX);
#pragma unroll
    for (int k = 0; k < CH; ++k) gv[k] = __builtin_amdgcn_raw_buffer_load_b128(gs, sub * 16 + k * 128, 0, 0);
    unsigned long long t2 = stamp();
    asm volatile("s_waitcnt vmcnt(13)" : "+v"(v[0]) :: "memory");
    unsigned long long t3 = stamp();
    asm volatile("s_waitcnt vmcnt(0)" : "+v"(v[0]), "+v"(v[1]), "+v"(v[2]), "+v"(v[3]), "+v"(v[4]), "+v"(v[5]), "+v"(v[6]) :: "memory");
    unsigned long long t4 = stamp();
    float ss0 = 0.f, ss1 = 0.f;
#pragma unroll
    for (int k = 0; k < CH; ++k) {
#pragma unroll
        for (int j = 0; j < 4; ++j) {
            float a = bf_lo(v[k][j]), b = bf_hi(v[k][j]);
            ss0 = fmaf(a, a, ss0);
            ss1 = fmaf(b, b, ss1);
        }
    }
    float ss = ss0 + ss1;
    ss = dpp_add<0xB1>(ss);
    ss = dpp_add<0x4E>(ss);
    ss = dpp_add<0x141>(ss);
    const float inv = __builtin_amdgcn_rsqf(ss * (1.0f / D) + 1e-6f);
#pragma unroll
    for (int k = 0; k < CH; ++k) {
        u32x4 o;
#pragma unroll
        for (int j = 0; j < 4; ++j) {
            float a = bf_lo(v[k][j]) * inv * bf_lo(gv[k][j]);
            float b = bf_hi(v[k][j]) * inv * bf_hi(gv[k][j]);
            o[j] = pack_bf16(a, b);
        }
        __builtin_amdgcn_raw_buffer_store_b128(o, os, off + k * 128, 0, ST_AUX);
    }
    unsigned long long t5 = stamp();
    asm volatile("s_waitcnt vmcnt(0)" ::: "memory");
    unsigned long long t6 = stamp();
    if ((threadIdx.x & 63) == 0) {
        unsigned long long* p = dbg + (size_t)(tid >> 6) * NS;
        p[0] = t0; p[1] = t1; p[2] = t2; p[3] = t3; p[4] = t4; p[5] = t5; p[6] = t6; p[7] = 0;
    }
}

__global__ __launch_bounds__(1024) void spin_kernel(const unsigned long long* __restrict__ dbg, int nw, int qsel, int kmul) {
    unsigned long long tent = stamp();
    __shared__ unsigned long long smin[16], smax[16], ssum[16];
    unsigned long long mn = ~0ull, mx = 0, sm = 0;
    const int s = qsel == 0 ? 0 : (qsel <= 7 ? qsel - 1 : (qsel <= 17 ? qsel - 11 : 6));
    for (int w = threadIdx.x; w < nw; w += 1024) {
        unsigned long long a = dbg[w * NS], b = dbg[w * NS + s];
        mn = a < mn ? a : mn; mx = b > mx ? b : mx; sm += (b & 0xffffffffffull);
    }
    for (int d = 32; d > 0; d >>= 1) {
        unsigned long long a = __shfl_xor(mn, d, 64), b = __shfl_xor(mx, d, 64), c = __shfl_xor(sm, d, 64);
        mn = a < mn ? a : mn; mx = b > mx ? b : mx; sm += c;
    }
    if ((threadIdx.x & 63) == 0) { smin[threadIdx.x >> 6] = mn; smax[threadIdx.x >> 6] = mx; ssum[threadIdx.x >> 6] = sm; }
    __syncthreads();
    if (threadIdx.x == 0) {
        sm = 0;
        for (int i = 0; i < 16; ++i) { mn = smin[i] < mn ? smin[i] : mn; mx = smax[i] > mx ? smax[i] : mx; sm += ssum[i]; }
        unsigned long long now = stamp();
        unsigned long long q = 0;
        if (qsel >= 1 && qsel <= 7) q = mx - mn;
        else if (qsel >= 11 && qsel <= 17) q = sm / nw - (mn & 0xffffffffffull);
        else if (qsel == 20) q = tent - mx;
        else if (qsel == 21) q = now - mn;
        else if (qsel == 22) q = now - tent;
        if (q > 3000) q = 3000;
        now = stamp();
        unsigned long long until = now + q * kmul;
        while (__builtin_amdgcn_s_memrealtime() < until) __builtin_amdgcn_s_sleep(1);
    }
}

extern "C" void kernel_launch(void* const* d_in, const int* in_sizes, int n_in,
                              void* d_out, int out_size, void* d_ws, size_t ws_size,
                              hipStream_t stream) {
    int rows = in_sizes[0] / D;
    int threads = rows * 8;
    int grid = (threads + TPB - 1) / TPB;
    tl_kernel<<<grid, TPB, 0, stream>>>(d_in[0], d_in[1], d_out, rows * D * 2, (unsigned long long*)d_ws);
    spin_kernel<<<1, 1024, 0, stream>>>((const unsigned long long*)d_ws, rows / 8, QSEL, KMUL);
}
```

```cpp
#include <hip/hip_runtime.h>
#include <stdint.h>

#define LD_AUX @LD_AUX@
#define ST_AUX @ST_AUX@
#define TPB @TPB@

typedef unsigned int u32x4 __attribute__((ext_vector_type(4)));
typedef float f32x2 __attribute__((ext_vector_type(2)));
typedef __bf16 bf16x2 __attribute__((ext_vector_type(2)));

constexpr int D = 448;
constexpr int CH = 7;

__device__ __forceinline__ float bf_lo(unsigned int u) { return __uint_as_float(u << 16); }
__device__ __forceinline__ float bf_hi(unsigned int u) { return __uint_as_float(u & 0xffff0000u); }
__device__ __forceinline__ unsigned int pack_bf16(float lo, float hi) {
    f32x2 f = {lo, hi};
    bf16x2 b = __builtin_convertvector(f, bf16x2);
    return __builtin_bit_cast(unsigned int, b);
}
template <int CTRL>
__device__ __forceinline__ float dpp_add(float v) {
    int t = __builtin_amdgcn_update_dpp(0, __float_as_int(v), CTRL, 0xf, 0xf, true);
    return v + __int_as_float(t);
}
__device__ __forceinline__ float dot2bf(unsigned int a, float c) {
    bf16x2 v = __builtin_bit_cast(bf16x2, a);
    return __builtin_amdgcn_fdot2_f32_bf16(v, v, c, false);
}
#define PIN(x) asm volatile("" : "+v"(x))

__global__ __launch_bounds__(TPB) void rmsnorm448_kernel(const void* __restrict__ x, const void* __restrict__ g,
                                                         void* __restrict__ out, int nbytes) {
    __shared__ u32x4 gsh[TPB];
    const int lane = threadIdx.x & 63;
    const int wave = (blockIdx.x * TPB + threadIdx.x) >> 6;
    auto gs = __builtin_amdgcn_make_buffer_rsrc((void*)g, 0, D * 2, 0x00020000);
    auto xs = __builtin_amdgcn_make_buffer_rsrc((void*)x, 0, nbytes, 0x00020000);
    auto os = __builtin_amdgcn_make_buffer_rsrc(out, 0, nbytes, 0x00020000);
    const int off = wave * (8 * D * 2) + (lane >> 3) * (D * 2) + (lane & 7) * 16;
    u32x4 gl = __builtin_amdgcn_raw_buffer_load_b128(gs, lane * 16, 0, 0);
    u32x4 v[CH];
#pragma unroll
    for (int k = 0; k < CH; ++k) v[k] = __builtin_amdgcn_raw_buffer_load_b128(xs, off + k * 128, 0, LD_AUX);
    __builtin_amdgcn_sched_barrier(0);
    gsh[threadIdx.x] = gl;
    u32x4 gv[CH];
    const u32x4* gp = gsh + (threadIdx.x & ~63) + (lane & 7);
#pragma unroll
    for (int k = 0; k < CH; ++k) gv[k] = gp[k * 8];
    f32x2 gf[CH][4];
#pragma unroll
    for (int k = 0; k < CH; ++k)
#pragma unroll
        for (int j = 0; j < 4; ++j) { gf[k][j] = f32x2{bf_lo(gv[k][j]), bf_hi(gv[k][j])}; PIN(gf[k][j]); }
    __builtin_amdgcn_sched_barrier(0);
    float ss0 = 0.f, ss1 = 0.f;
    f32x2 xg[CH][4];
#pragma unroll
    for (int k = 0; k < CH - 1; ++k) {
#pragma unroll
        for (int j = 0; j < 4; ++j) {
            if (j & 1) ss1 = dot2bf(v[k][j], ss1); else ss0 = dot2bf(v[k][j], ss0);
        }
        PIN(ss0); PIN(ss1);
#pragma unroll
        for (int j = 0; j < 4; ++j) {
            f32x2 a = {bf_lo(v[k][j]), bf_hi(v[k][j])};
            xg[k][j] = a * gf[k][j];
            PIN(xg[k][j]);
        }
        __builtin_amdgcn_sched_barrier(0);
    }
#pragma unroll
    for (int j = 0; j < 4; ++j) {
        if (j & 1) ss1 = dot2bf(v[CH - 1][j], ss1); else ss0 = dot2bf(v[CH - 1][j], ss0);
    }
    float ss = ss0 + ss1;
    ss = dpp_add<0xB1>(ss);
    ss = dpp_add<0x4E>(ss);
    ss = dpp_add<0x141>(ss);
    const float inv = __builtin_amdgcn_rsqf(ss * (1.0f / D) + 1e-6f);
#pragma unroll
    for (int j = 0; j < 4; ++j) {
        f32x2 a = {bf_lo(v[CH - 1][j]), bf_hi(v[CH - 1][j])};
        xg[CH - 1][j] = a * gf[CH - 1][j];
    }
    __builtin_amdgcn_sched_barrier(0);
#pragma unroll
    for (int k = 0; k < CH; ++k) {
        u32x4 o;
#pragma unroll
        for (int j = 0; j < 4; ++j) {
            f32x2 r = xg[k][j] * inv;
            o[j] = pack_bf16(r.x, r.y);
        }
        __builtin_amdgcn_raw_buffer_store_b128(o, os, off + k * 128, 0, ST_AUX);
    }
}

extern "C" void kernel_launch(void* const* d_in, const int* in_sizes, int n_in,
                              void* d_out, int out_size, void* d_ws, size_t ws_size,
                              hipStream_t stream) {
    int rows = in_sizes[0] / D;
    int waves = (rows + 7) / 8;
    int grid = (waves * 64 + TPB - 1) / TPB;
    rmsnorm448_kernel<<<grid, TPB, 0, stream>>>(d_in[0], d_in[1], d_out, rows * D * 2);
}
```

```cpp
#include <hip/hip_runtime.h>
#include <stdint.h>

#define LD_AUX @LD_AUX@
#define ST_A @ST_A@
#define ST_B @ST_B@
#define SPLIT_NUM @SPLIT_NUM@
#define TPB @TPB@

typedef unsigned int u32x4 __attribute__((ext_vector_type(4)));
typedef float f32x2 __attribute__((ext_vector_type(2)));
typedef __bf16 bf16x2 __attribute__((ext_vector_type(2)));

constexpr int D = 448;
constexpr int CH = 7;

__device__ __forceinline__ float bf_lo(unsigned int u) { return __uint_as_float(u << 16); }
__device__ __forceinline__ float bf_hi(unsigned int u) { return __uint_as_float(u & 0xffff0000u); }
__device__ __forceinline__ unsigned int pack_bf16(float lo, float hi) {
    f32x2 f = {lo, hi};
    bf16x2 b = __builtin_convertvector(f, bf16x2);
    return __builtin_bit_cast(unsigned int, b);
}
template <int CTRL>
__device__ __forceinline__ float dpp_add(float v) {
    int t = __builtin_amdgcn_update_dpp(0, __float_as_int(v), CTRL, 0xf, 0xf, true);
    return v + __int_as_float(t);
}
__device__ __forceinline__ float dot2bf(unsigned int a, float c) {
    bf16x2 v = __builtin_bit_cast(bf16x2, a);
    return __builtin_amdgcn_fdot2_f32_bf16(v, v, c, false);
}
#define PIN(x) asm volatile("" : "+v"(x))

__global__ __launch_bounds__(TPB) void rmsnorm448_kernel(const void* __restrict__ x, const void* __restrict__ g,
                                                         void* __restrict__ out, int nbytes, int split) {
    __shared__ u32x4 gsh[TPB];
    const int lane = threadIdx.x & 63;
    const int wave = (blockIdx.x * TPB + threadIdx.x) >> 6;
    auto gs = __builtin_amdgcn_make_buffer_rsrc((void*)g, 0, D * 2, 0x00020000);
    auto xs = __builtin_amdgcn_make_buffer_rsrc((void*)x, 0, nbytes, 0x00020000);
    auto os = __builtin_amdgcn_make_buffer_rsrc(out, 0, nbytes, 0x00020000);
    const int off = wave * (8 * D * 2) + (lane >> 3) * (D * 2) + (lane & 7) * 16;
    u32x4 gl = __builtin_amdgcn_raw_buffer_load_b128(gs, lane * 16, 0, 0);
    u32x4 v[CH];
#pragma unroll
    for (int k = 0; k < CH; ++k) v[k] = __builtin_amdgcn_raw_buffer_load_b128(xs, off + k * 128, 0, LD_AUX);
    __builtin_amdgcn_sched_barrier(0);
    gsh[threadIdx.x] = gl;
    u32x4 gv[CH];
    const u32x4* gp = gsh + (threadIdx.x & ~63) + (lane & 7);
#pragma unroll
    for (int k = 0; k < CH; ++k) gv[k] = gp[k * 8];
    f32x2 gf[CH][4];
#pragma unroll
    for (int k = 0; k < CH; ++k)
#pragma unroll
        for (int j = 0; j < 4; ++j) { gf[k][j] = f32x2{bf_lo(gv[k][j]), bf_hi(gv[k][j])}; PIN(gf[k][j]); }
    __builtin_amdgcn_sched_barrier(0);
    float ss0 = 0.f, ss1 = 0.f;
    f32x2 xg[CH][4];
#pragma unroll
    for (int k = 0; k < CH - 1; ++k) {
#pragma unroll
        for (int j = 0; j < 4; ++j) {
            if (j & 1) ss1 = dot2bf(v[k][j], ss1); else ss0 = dot2bf(v[k][j], ss0);
        }
        PIN(ss0); PIN(ss1);
#pragma unroll
        for (int j = 0; j < 4; ++j) {
            f32x2 a = {bf_lo(v[k][j]), bf_hi(v[k][j])};
            xg[k][j] = a * gf[k][j];
            PIN(xg[k][j]);
        }
        __builtin_amdgcn_sched_barrier(0);
    }
#pragma unroll
    for (int j = 0; j < 4; ++j) {
        if (j & 1) ss1 = dot2bf(v[CH - 1][j], ss1); else ss0 = dot2bf(v[CH - 1][j], ss0);
    }
    float ss = ss0 + ss1;
    ss = dpp_add<0xB1>(ss);
    ss = dpp_add<0x4E>(ss);
    ss = dpp_add<0x141>(ss);
    const float inv = __builtin_amdgcn_rsqf(ss * (1.0f / D) + 1e-6f);
#pragma unroll
    for (int j = 0; j < 4; ++j) {
        f32x2 a = {bf_lo(v[CH - 1][j]), bf_hi(v[CH - 1][j])};
        xg[CH - 1][j] = a * gf[CH - 1][j];
    }
    __builtin_amdgcn_sched_barrier(0);
    u32x4 o[CH];
#pragma unroll
    for (int k = 0; k < CH; ++k)
#pragma unroll
        for (int j = 0; j < 4; ++j) {
            f32x2 r = xg[k][j] * inv;
            o[k][j] = pack_bf16(r.x, r.y);
        }
    if ((int)blockIdx.x < split) {
#pragma unroll
        for (int k = 0; k < CH; ++k) __builtin_amdgcn_raw_buffer_store_b128(o[k], os, off + k * 128, 0, ST_A);
    } else {
#pragma unroll
        for (int k = 0; k < CH; ++k) __builtin_amdgcn_raw_buffer_store_b128(o[k], os, off + k * 128, 0, ST_B);
    }
}

extern "C" void kernel_launch(void* const* d_in, const int* in_sizes, int n_in,
                              void* d_out, int out_size, void* d_ws, size_t ws_size,
                              hipStream_t stream) {
    int rows = in_sizes[0] / D;
    int waves = (rows + 7) / 8;
    int grid = (waves * 64 + TPB - 1) / TPB;
    rmsnorm448_kernel<<<grid, TPB, 0, stream>>>(d_in[0], d_in[1], d_out, rows * D * 2, grid * SPLIT_NUM / 16);
}
```

```cpp
#include <hip/hip_runtime.h>
#include <hip/hip_bf16.h>
#include <stdint.h>

#define LD_AUX @LD_AUX@
#define ST_AUX @ST_AUX@
#define TPB 256
#define QSEL @QSEL@
#define KMUL @KMUL@

typedef unsigned int u32x4 __attribute__((ext_vector_type(4)));
typedef float f32x2 __attribute__((ext_vector_type(2)));
typedef __bf16 bf16x2 __attribute__((ext_vector_type(2)));
constexpr int D = 448;
constexpr int CH = 7;
__device__ __forceinline__ float bf_lo(unsigned int u) { return __uint_as_float(u << 16); }
__device__ __forceinline__ float bf_hi(unsigned int u) { return __uint_as_float(u & 0xffff0000u); }
__device__ __forceinline__ unsigned int pack_bf16(float lo, float hi) {
    f32x2 f = {lo, hi};
    bf16x2 b = __builtin_convertvector(f, bf16x2);
    return __builtin_bit_cast(unsigned int, b);
}
template <int CTRL>
__device__ __forceinline__ float dpp_add(float v) {
    int t = __builtin_amdgcn_update_dpp(0, __float_as_int(v), CTRL, 0xf, 0xf, true);
    return v + __int_as_float(t);
}
__device__ __forceinline__ unsigned long long stamp() {
    unsigned long long t;
    __builtin_amdgcn_sched_barrier(0);
    asm volatile("s_memrealtime %0\n\ts_waitcnt lgkmcnt(0)" : "=s"(t) :: "memory");
    __builtin_amdgcn_sched_barrier(0);
    return t;
}
constexpr int NS = 8;
__global__ __launch_bounds__(TPB) void tl_kernel(const void* __restrict__ x, const void* __restrict__ g,
                                                 void* __restrict__ out, int nbytes, unsigned long long* __restrict__ dbg) {
    unsigned long long t0 = stamp();
    const int tid = blockIdx.x * TPB + threadIdx.x;
    const int row = tid >> 3;
    const int sub = tid & 7;
    auto xs = __builtin_amdgcn_make_buffer_rsrc((void*)x, 0, nbytes, 0x00020000);
    auto gs = __builtin_amdgcn_make_buffer_rsrc((void*)g, 0, D * 2, 0x00020000);
    auto os = __builtin_amdgcn_make_buffer_rsrc(out, 0, nbytes, 0x00020000);
    const int off = row * (D * 2) + sub * 16;
    asm volatile("" :: "s"(x), "s"(out), "s"(g));
    unsigned long long t1 = stamp();
    u32x4 v[CH], gv[CH];
#pragma unroll
    for (int k = 0; k < CH; ++k) v[k] = __builtin_amdgcn_raw_buffer_load_b128(xs, off + k * 128, 0, LD_AUX);
#pragma unroll
    for (int k = 0; k < CH; ++k) gv[k] = __builtin_amdgcn_raw_buffer_load_b128(gs, sub * 16 + k * 128, 0, 0);
    unsigned long long t2 = stamp();
    asm volatile("s_waitcnt vmcnt(13)" : "+v"(v[0]) :: "memory");
    unsigned long long t3 = stamp();
    asm volatile("s_waitcnt vmcnt(0)" : "+v"(v[0]), "+v"(v[1]), "+v"(v[2]), "+v"(v[3]), "+v"(v[4]), "+v"(v[5]), "+v"(v[6]) :: "memory");
    unsigned long long t4 = stamp();
    float ss0 = 0.f, ss1 = 0.f;
#pragma unroll
    for (int k = 0; k < CH; ++k) {
#pragma unroll
        for (int j = 0; j < 4; ++j) {
            float a = bf_lo(v[k][j]), b = bf_hi(v[k][j]);
            ss0 = fmaf(a, a, ss0);
            ss1 = fmaf(b, b, ss1);
        }
    }
    float ss = ss0 + ss1;
    ss = dpp_add<0xB1>(ss);
    ss = dpp_add<0x4E>(ss);
    ss = dpp_add<0x141>(ss);
    const float inv = __builtin_amdgcn_rsqf(ss * (1.0f / D) + 1e-6f);
#pragma unroll
    for (int k = 0; k < CH; ++k) {
        u32x4 o;
#pragma unroll
        for (int j = 0; j < 4; ++j) {
            float a = bf_lo(v[k][j]) * inv * bf_lo(gv[k][j]);
            float b = bf_hi(v[k][j]) * inv * bf_hi(gv[k][j]);
            o[j] = pack_bf16(a, b);
        }
        __builtin_amdgcn_raw_buffer_store_b128(o, os, off + k * 128, 0, ST_AUX);
    }
    unsigned long long t5 = stamp();
    asm volatile("s_waitcnt vmcnt(0)" ::: "memory");
    unsigned long long t6 = stamp();
    if ((threadIdx.x & 63) == 0) {
        unsigned long long* p = dbg + (size_t)(tid >> 6) * NS;
        p[0] = t0; p[1] = t1; p[2] = t2; p[3] = t3; p[4] = t4; p[5] = t5; p[6] = t6; p[7] = (unsigned)__builtin_amdgcn_s_getreg(20 | (3 << 11)) & 0xfu;
    }
}

__global__ __launch_bounds__(1024) void spin_kernel(const unsigned long long* __restrict__ dbg, int nw, int qsel, int kmul) {
    unsigned long long tent = stamp();
    __shared__ unsigned long long smin[16], smax[16], ssum[16];
    unsigned long long mn = ~0ull, mx = 0, sm = 0;
    const int s = qsel == 0 ? 0 : (qsel <= 7 ? qsel - 1 : (qsel <= 17 ? qsel - 11 : 6));
    for (int w = threadIdx.x; w < nw; w += 1024) {
        unsigned long long a = dbg[w * NS], b = dbg[w * NS + s];
        unsigned long long xc = dbg[w * NS + 7];
        mn = a < mn ? a : mn;
        if (qsel >= 30 && qsel < 38) { if (xc == (unsigned long long)(qsel - 30)) mx = (~a) > mx ? (~a) : mx; }
        else if (qsel >= 40 && qsel < 48) { if (xc == (unsigned long long)(qsel - 40)) mx = b > mx ? b : mx; }
        else mx = b > mx ? b : mx;
        sm += (b & 0xffffffffffull);
    }
    for (int d = 32; d > 0; d >>= 1) {
        unsigned long long a = __shfl_xor(mn, d, 64), b = __shfl_xor(mx, d, 64), c = __shfl_xor(sm, d, 64);
        mn = a < mn ? a : mn; mx = b > mx ? b : mx; sm += c;
    }
    if ((threadIdx.x & 63) == 0) { smin[threadIdx.x >> 6] = mn; smax[threadIdx.x >> 6] = mx; ssum[threadIdx.x >> 6] = sm; }
    __syncthreads();
    if (threadIdx.x == 0) {
        sm = 0;
        for (int i = 0; i < 16; ++i) { mn = smin[i] < mn ? smin[i] : mn; mx = smax[i] > mx ? smax[i] : mx; sm += ssum[i]; }
        unsigned long long now = stamp();
        unsigned long long q = 0;
        if (qsel >= 1 && qsel <= 7) q = mx - mn;
        else if (qsel >= 11 && qsel <= 17) q = sm / nw - (mn & 0xffffffffffull);
        else if (qsel >= 30 && qsel < 38) q = (~mx) - mn;
        else if (qsel >= 40 && qsel < 48) q = mx - mn;
        else if (qsel == 50) q = 20 * (dbg[7] + 8 * ((dbg[4 * NS + 7] - dbg[7]) & 7)) + 10;
        else if (qsel == 20) q = tent - mx;
        else if (qsel == 21) q = now - mn;
        else if (qsel == 22) q = now - tent;
        if (q > 3000) q = 3000;
        now = stamp();
        unsigned long long until = now + q * kmul;
        while (__builtin_amdgcn_s_memrealtime() < until) __builtin_amdgcn_s_sleep(1);
    }
}

extern "C" void kernel_launch(void* const* d_in, const int* in_sizes, int n_in,
                              void* d_out, int out_size, void* d_ws, size_t ws_size,
                              hipStream_t stream) {
    int rows = in_sizes[0] / D;
    int threads = rows * 8;
    int grid = (threads + TPB - 1) / TPB;
    tl_kernel<<<grid, TPB, 0, stream>>>(d_in[0], d_in[1], d_out, rows * D * 2, (unsigned long long*)d_ws);
    spin_kernel<<<1, 1024, 0, stream>>>((const unsigned long long*)d_ws, rows / 8, QSEL, KMUL);
}
```

```cpp
#include <hip/hip_runtime.h>
#include <stdint.h>

#define LD_AUX @LD_AUX@
#define ST_AUX @ST_AUX@
#define TPB @TPB@
#define SHG @SHG@
#define PRIO @PRIO@

typedef unsigned int u32x4 __attribute__((ext_vector_type(4)));
typedef float f32x2 __attribute__((ext_vector_type(2)));
typedef __bf16 bf16x2 __attribute__((ext_vector_type(2)));

constexpr int D = 448;
constexpr int CH = 7;

__device__ __forceinline__ float bf_lo(unsigned int u) { return __uint_as_float(u << 16); }
__device__ __forceinline__ float bf_hi(unsigned int u) { return __uint_as_float(u & 0xffff0000u); }
__device__ __forceinline__ unsigned int pack_bf16(float lo, float hi) {
    f32x2 f = {lo, hi};
    bf16x2 b = __builtin_convertvector(f, bf16x2);
    return __builtin_bit_cast(unsigned int, b);
}
template <int CTRL>
__device__ __forceinline__ float dpp_add(float v) {
    int t = __builtin_amdgcn_update_dpp(0, __float_as_int(v), CTRL, 0xf, 0xf, true);
    return v + __int_as_float(t);
}
__device__ __forceinline__ float dot2bf(unsigned int a, float c) {
    bf16x2 v = __builtin_bit_cast(bf16x2, a);
    return __builtin_amdgcn_fdot2_f32_bf16(v, v, c, false);
}
#define PIN(x) asm volatile("" : "+v"(x))

__global__ __launch_bounds__(TPB) void rmsnorm448_kernel(const void* __restrict__ x, const void* __restrict__ g,
                                                         void* __restrict__ out, int nbytes) {
    __shared__ u32x4 gsh[TPB];
    const int lane = threadIdx.x & 63;
    const int wave = (blockIdx.x * TPB + threadIdx.x) >> 6;
    auto gs = __builtin_amdgcn_make_buffer_rsrc((void*)g, 0, D * 2, 0x00020000);
    auto xs = __builtin_amdgcn_make_buffer_rsrc((void*)x, 0, nbytes, 0x00020000);
    auto os = __builtin_amdgcn_make_buffer_rsrc(out, 0, nbytes, 0x00020000);
    const int off = wave * (8 * D * 2) + (lane >> 3) * (D * 2) + (lane & 7) * 16;
#if PRIO
    __builtin_amdgcn_s_setprio(3);
#endif
#if SHG
    u32x4 gl;
    const bool gw = __builtin_amdgcn_readfirstlane(threadIdx.x >> 6) == 0;
    if (gw) gl = __builtin_amdgcn_raw_buffer_load_b128(gs, lane * 16, 0, 0);
#else
    u32x4 gl = __builtin_amdgcn_raw_buffer_load_b128(gs, lane * 16, 0, 0);
#endif
    u32x4 v[CH];
#pragma unroll
    for (int k = 0; k < CH; ++k) v[k] = __builtin_amdgcn_raw_buffer_load_b128(xs, off + k * 128, 0, LD_AUX);
    __builtin_amdgcn_sched_barrier(0);
#if PRIO
    __builtin_amdgcn_s_setprio(0);
#endif
    u32x4 gv[CH];
#if SHG
    if (gw) gsh[lane] = gl;
    __syncthreads();
    const u32x4* gp = gsh + (lane & 7);
#else
    gsh[threadIdx.x] = gl;
    const u32x4* gp = gsh + (threadIdx.x & ~63) + (lane & 7);
#endif
#pragma unroll
    for (int k = 0; k < CH; ++k) gv[k] = gp[k * 8];
    f32x2 gf[CH][4];
#pragma unroll
    for (int k = 0; k < CH; ++k)
#pragma unroll
        for (int j = 0; j < 4; ++j) { gf[k][j] = f32x2{bf_lo(gv[k][j]), bf_hi(gv[k][j])}; PIN(gf[k][j]); }
    __builtin_amdgcn_sched_barrier(0);
    float ss0 = 0.f, ss1 = 0.f;
    f32x2 xg[CH][4];
#pragma unroll
    for (int k = 0; k < CH - 1; ++k) {
#pragma unroll
        for (int j = 0; j < 4; ++j) {
            if (j & 1) ss1 = dot2bf(v[k][j], ss1); else ss0 = dot2bf(v[k][j], ss0);
        }
        PIN(ss0); PIN(ss1);
#pragma unroll
        for (int j = 0; j < 4; ++j) {
            f32x2 a = {bf_lo(v[k][j]), bf_hi(v[k][j])};
            xg[k][j] = a * gf[k][j];
            PIN(xg[k][j]);
        }
        __builtin_amdgcn_sched_barrier(0);
    }
#pragma unroll
    for (int j = 0; j < 4; ++j) {
        if (j & 1) ss1 = dot2bf(v[CH - 1][j], ss1); else ss0 = dot2bf(v[CH - 1][j], ss0);
    }
    float ss = ss0 + ss1;
    ss = dpp_add<0xB1>(ss);
    ss = dpp_add<0x4E>(ss);
    ss = dpp_add<0x141>(ss);
    const float inv = __builtin_amdgcn_rsqf(ss * (1.0f / D) + 1e-6f);
#pragma unroll
    for (int j = 0; j < 4; ++j) {
        f32x2 a = {bf_lo(v[CH - 1][j]), bf_hi(v[CH - 1][j])};
        xg[CH - 1][j] = a * gf[CH - 1][j];
    }
    __builtin_amdgcn_sched_barrier(0);
#pragma unroll
    for (int k = 0; k < CH; ++k) {
        u32x4 o;
#pragma unroll
        for (int j = 0; j < 4; ++j) {
            f32x2 r = xg[k][j] * inv;
            o[j] = pack_bf16(r.x, r.y);
        }
        __builtin_amdgcn_raw_buffer_store_b128(o, os, off + k * 128, 0, ST_AUX);
    }
}

extern "C" void kernel_launch(void* const* d_in, const int* in_sizes, int n_in,
                              void* d_out, int out_size, void* d_ws, size_t ws_size,
                              hipStream_t stream) {
    int rows = in_sizes[0] / D;
    int waves = (rows + 7) / 8;
    int grid = (waves * 64 + TPB - 1) / TPB;
    rmsnorm448_kernel<<<grid, TPB, 0, stream>>>(d_in[0], d_in[1], d_out, rows * D * 2);
}
```

```cpp
#include <hip/hip_runtime.h>
#include <hip/hip_bf16.h>
#include <stdint.h>

#define LD_AUX @LD_AUX@
#define ST_AUX @ST_AUX@
#define TPB @TPB@
#define QSEL @QSEL@
#define KMUL @KMUL@

typedef unsigned int u32x4 __attribute__((ext_vector_type(4)));
typedef float f32x2 __attribute__((ext_vector_type(2)));
typedef __bf16 bf16x2 __attribute__((ext_vector_type(2)));
constexpr int D = 448;
constexpr int CH = 7;
__device__ __forceinline__ float bf_lo(unsigned int u) { return __uint_as_float(u << 16); }
__device__ __forceinline__ float bf_hi(unsigned int u) { return __uint_as_float(u & 0xffff0000u); }
__device__ __forceinline__ unsigned int pack_bf16(float lo, float hi) {
    f32x2 f = {lo, hi};
    bf16x2 b = __builtin_convertvector(f, bf16x2);
    return __builtin_bit_cast(unsigned int, b);
}
template <int CTRL>
__device__ __forceinline__ float dpp_add(float v) {
    int t = __builtin_amdgcn_update_dpp(0, __float_as_int(v), CTRL, 0xf, 0xf, true);
    return v + __int_as_float(t);
}
__device__ __forceinline__ unsigned long long stamp() {
    unsigned long long t;
    __builtin_amdgcn_sched_barrier(0);
    asm volatile("s_memrealtime %0\n\ts_waitcnt lgkmcnt(0)" : "=s"(t) :: "memory");
    __builtin_amdgcn_sched_barrier(0);
    return t;
}
constexpr int NS = 8;
__global__ __launch_bounds__(TPB) void tl_kernel(const void* __restrict__ x, const void* __restrict__ g,
                                                 void* __restrict__ out, int nbytes, unsigned long long* __restrict__ dbg) {
    unsigned long long t0 = stamp();
    const int tid = blockIdx.x * TPB + threadIdx.x;
    const int row = tid >> 3;
    const int sub = tid & 7;
    auto xs = __builtin_amdgcn_make_buffer_rsrc((void*)x, 0, nbytes, 0x00020000);
    auto gs = __builtin_amdgcn_make_buffer_rsrc((void*)g, 0, D * 2, 0x00020000);
    auto os = __builtin_amdgcn_make_buffer_rsrc(out, 0, nbytes, 0x00020000);
    const int off = row * (D * 2) + sub * 16;
    asm volatile("" :: "s"(x), "s"(out), "s"(g));
    unsigned long long t1 = stamp();
    u32x4 v[CH], gv[CH];
#pragma unroll
    for (int k = 0; k < CH; ++k) v[k] = __builtin_amdgcn_raw_buffer_load_b128(xs, off + k * 128, 0, LD_AUX);
#pragma unroll
    for (int k = 0; k < CH; ++k) gv[k] = __builtin_amdgcn_raw_buffer_load_b128(gs, sub * 16 + k * 128, 0, 0);
    unsigned long long t2 = stamp();
    asm volatile("s_waitcnt vmcnt(13)" : "+v"(v[0]) :: "memory");
    unsigned long long t3 = stamp();
    asm volatile("s_waitcnt vmcnt(0)" : "+v"(v[0]), "+v"(v[1]), "+v"(v[2]), "+v"(v[3]), "+v"(v[4]), "+v"(v[5]), "+v"(v[6]) :: "memory");
    unsigned long long t4 = stamp();
    float ss0 = 0.f, ss1 = 0.f;
#pragma unroll
    for (int k = 0; k < CH; ++k) {
#pragma unroll
        for (int j = 0; j < 4; ++j) {
            float a = bf_lo(v[k][j]), b = bf_hi(v[k][j]);
            ss0 = fmaf(a, a, ss0);
            ss1 = fmaf(b, b, ss1);
        }
    }
    float ss = ss0 + ss1;
    ss = dpp_add<0xB1>(ss);
    ss = dpp_add<0x4E>(ss);
    ss = dpp_add<0x141>(ss);
    const float inv = __builtin_amdgcn_rsqf(ss * (1.0f / D) + 1e-6f);
#pragma unroll
    for (int k = 0; k < CH; ++k) {
        u32x4 o;
#pragma unroll
        for (int j = 0; j < 4; ++j) {
            float a = bf_lo(v[k][j]) * inv * bf_lo(gv[k][j]);
            float b = bf_hi(v[k][j]) * inv * bf_hi(gv[k][j]);
            o[j] = pack_bf16(a, b);
        }
        __builtin_amdgcn_raw_buffer_store_b128(o, os, off + k * 128, 0, ST_AUX);
    }
    unsigned long long t5 = stamp();
    asm volatile("s_waitcnt vmcnt(0)" ::: "memory");
    unsigned long long t6 = stamp();
    if ((threadIdx.x & 63) == 0) {
        unsigned long long* p = dbg + (size_t)(tid >> 6) * NS;
        p[0] = t0; p[1] = t1; p[2] = t2; p[3] = t3; p[4] = t4; p[5] = t5; p[6] = t6; p[7] = (unsigned)__builtin_amdgcn_s_getreg(20 | (3 << 11)) & 0xfu;
    }
}

__global__ __launch_bounds__(1024) void spin_kernel(const unsigned long long* __restrict__ dbg, int nw, int qsel, int kmul) {
    unsigned long long tent = stamp();
    __shared__ unsigned long long smin[16], smax[16], ssum[16];
    unsigned long long mn = ~0ull, mx = 0, sm = 0;
    const int s = qsel == 0 ? 0 : (qsel <= 7 ? qsel - 1 : (qsel <= 17 ? qsel - 11 : 6));
    for (int w = threadIdx.x; w < nw; w += 1024) {
        unsigned long long a = dbg[w * NS], b = dbg[w * NS + s];
        unsigned long long xc = dbg[w * NS + 7];
        mn = a < mn ? a : mn;
        if (qsel >= 30 && qsel < 38) { if (xc == (unsigned long long)(qsel - 30)) mx = (~a) > mx ? (~a) : mx; }
        else if (qsel >= 40 && qsel < 48) { if (xc == (unsigned long long)(qsel - 40)) mx = b > mx ? b : mx; }
        else mx = b > mx ? b : mx;
        sm += (b & 0xffffffffffull);
    }
    for (int d = 32; d > 0; d >>= 1) {
        unsigned long long a = __shfl_xor(mn, d, 64), b = __shfl_xor(mx, d, 64), c = __shfl_xor(sm, d, 64);
        mn = a < mn ? a : mn; mx = b > mx ? b : mx; sm += c;
    }
    if ((threadIdx.x & 63) == 0) { smin[threadIdx.x >> 6] = mn; smax[threadIdx.x >> 6] = mx; ssum[threadIdx.x >> 6] = sm; }
    __syncthreads();
    if (threadIdx.x == 0) {
        sm = 0;
        for (int i = 0; i < 16; ++i) { mn = smin[i] < mn ? smin[i] : mn; mx = smax[i] > mx ? smax[i] : mx; sm += ssum[i]; }
        unsigned long long now = stamp();
        unsigned long long q = 0;
        if (qsel >= 1 && qsel <= 7) q = mx - mn;
        else if (qsel >= 11 && qsel <= 17) q = sm / nw - (mn & 0xffffffffffull);
        else if (qsel >= 30 && qsel < 38) q = (~mx) - mn;
        else if (qsel >= 40 && qsel < 48) q = mx - mn;
        else if (qsel == 50) q = 20 * (dbg[7] + 8 * ((dbg[4 * NS + 7] - dbg[7]) & 7)) + 10;
        else if (qsel == 20) q = tent - mx;
        else if (qsel == 21) q = now - mn;
        else if (qsel == 22) q = now - tent;
        if (q > 3000) q = 3000;
        now = stamp();
        unsigned long long until = now + q * kmul;
        while (__builtin_amdgcn_s_memrealtime() < until) __builtin_amdgcn_s_sleep(1);
    }
}

extern "C" void kernel_launch(void* const* d_in, const int* in_sizes, int n_in,
                              void* d_out, int out_size, void* d_ws, size_t ws_size,
                              hipStream_t stream) {
    int rows = in_sizes[0] / D;
    int threads = rows * 8;
    int grid = (threads + TPB - 1) / TPB;
    tl_kernel<<<grid, TPB, 0, stream>>>(d_in[0], d_in[1], d_out, rows * D * 2, (unsigned long long*)d_ws);
    spin_kernel<<<1, 1024, 0, stream>>>((const unsigned long long*)d_ws, rows / 8, QSEL, KMUL);
}
```

```cpp
#include <hip/hip_runtime.h>
#include <stdint.h>

typedef unsigned int u32x4 __attribute__((ext_vector_type(4)));
typedef float f32x2 __attribute__((ext_vector_type(2)));
typedef __bf16 bf16x2 __attribute__((ext_vector_type(2)));

constexpr int D = 448;
constexpr int CH = 7;
constexpr int TPB = 512;
constexpr int NT = 2;

__device__ __forceinline__ float bf_lo(unsigned int u) { return __uint_as_float(u << 16); }
__device__ __forceinline__ float bf_hi(unsigned int u) { return __uint_as_float(u & 0xffff0000u); }
__device__ __forceinline__ unsigned int pack_bf16(float lo, float hi) {
    f32x2 f = {lo, hi};
    bf16x2 b = __builtin_convertvector(f, bf16x2);
    return __builtin_bit_cast(unsigned int, b);
}
template <int CTRL>
__device__ __forceinline__ float dpp_add(float v) {
    int t = __builtin_amdgcn_update_dpp(0, __float_as_int(v), CTRL, 0xf, 0xf, true);
    return v + __int_as_float(t);
}
__device__ __forceinline__ float dot2bf(unsigned int a, float c) {
    bf16x2 v = __builtin_bit_cast(bf16x2, a);
    return __builtin_amdgcn_fdot2_f32_bf16(v, v, c, false);
}
#define PIN(x) asm volatile("" : "+v"(x))

extern "C" __global__ __launch_bounds__(TPB) void rmsnorm448_kernel(const void* __restrict__ x, const void* __restrict__ g,
                                                                    void* __restrict__ out, int nbytes) {
    __shared__ u32x4 gsh[64];
    const int lane = threadIdx.x & 63;
    auto xs = __builtin_amdgcn_make_buffer_rsrc((void*)x, 0, nbytes, 0x00020000);
    auto gs = __builtin_amdgcn_make_buffer_rsrc((void*)g, 0, D * 2, 0x00020000);
    auto os = __builtin_amdgcn_make_buffer_rsrc(out, 0, nbytes, 0x00020000);
    const int off = (blockIdx.x * (TPB / 8) + (threadIdx.x >> 3)) * (D * 2) + (threadIdx.x & 7) * 16;
    const bool gw = __builtin_amdgcn_readfirstlane(threadIdx.x) == 0;
    u32x4 gl;
    if (gw) gl = __builtin_amdgcn_raw_buffer_load_b128(gs, lane * 16, 0, 0);
    u32x4 v[CH];
#pragma unroll
    for (int k = 0; k < CH; ++k) v[k] = __builtin_amdgcn_raw_buffer_load_b128(xs, off + k * 128, 0, NT);
    __builtin_amdgcn_sched_barrier(0);
    if (gw) gsh[lane] = gl;
    __syncthreads();
    u32x4 gv[CH];
#pragma unroll
    for (int k = 0; k < CH; ++k) gv[k] = gsh[k * 8 + (lane & 7)];
    f32x2 gf[CH][4];
#pragma unroll
    for (int k = 0; k < CH; ++k)
#pragma unroll
        for (int j = 0; j < 4; ++j) { gf[k][j] = f32x2{bf_lo(gv[k][j]), bf_hi(gv[k][j])}; PIN(gf[k][j]); }
    __builtin_amdgcn_sched_barrier(0);

    float ss0 = 0.f, ss1 = 0.f;
    f32x2 xg[CH][4];
#pragma unroll
    for (int k = 0; k < CH - 1; ++k) {
#pragma unroll
        for (int j = 0; j < 4; ++j) { if (j & 1) ss1 = dot2bf(v[k][j], ss1); else ss0 = dot2bf(v[k][j], ss0); }
        PIN(ss0); PIN(ss1);
#pragma unroll
        for (int j = 0; j < 4; ++j) {
            f32x2 a = {bf_lo(v[k][j]), bf_hi(v[k][j])};
            xg[k][j] = a * gf[k][j];
            PIN(xg[k][j]);
        }
        __builtin_amdgcn_sched_barrier(0);
    }
#pragma unroll
    for (int j = 0; j < 4; ++j) { if (j & 1) ss1 = dot2bf(v[CH - 1][j], ss1); else ss0 = dot2bf(v[CH - 1][j], ss0); }
    float ss = ss0 + ss1;
    ss = dpp_add<0xB1>(ss);
    ss = dpp_add<0x4E>(ss);
    ss = dpp_add<0x141>(ss);
    const float inv = __builtin_amdgcn_rsqf(ss * (1.0f / D) + 1e-6f);
#pragma unroll
    for (int j = 0; j < 4; ++j) {
        f32x2 a = {bf_lo(v[CH - 1][j]), bf_hi(v[CH - 1][j])};
        xg[CH - 1][j] = a * gf[CH - 1][j];
    }
    __builtin_amdgcn_sched_barrier(0);
#pragma unroll
    for (int k = 0; k < CH; ++k) {
        u32x4 o;
#pragma unroll
        for (int j = 0; j < 4; ++j) {
            f32x2 r = xg[k][j] * inv;
            o[j] = pack_bf16(r.x, r.y);
        }
        __builtin_amdgcn_raw_buffer_store_b128(o, os, off + k * 128, 0, NT);
    }
}

extern "C" void kernel_launch(void* const* d_in, const int* in_sizes, int n_in,
                              void* d_out, int out_size, void* d_ws, size_t ws_size,
                              hipStream_t stream) {
    const int rows = in_sizes[0] / D;
    const int grid = (rows + TPB / 8 - 1) / (TPB / 8);
    rmsnorm448_kernel<<<grid, TPB, 0, stream>>>(d_in[0], d_in[1], d_out, rows * D * 2);
}
```

```cpp
#include <hip/hip_runtime.h>
#include <stdint.h>

typedef unsigned int u32x4 __attribute__((ext_vector_type(4)));
typedef float f32x2 __attribute__((ext_vector_type(2)));
typedef __bf16 bf16x2 __attribute__((ext_vector_type(2)));

constexpr int D = 448;
constexpr int CH = 7;
constexpr int TPB = @TPB@;
constexpr int NB = @NB@;
constexpr int NT = 2;

__device__ __forceinline__ float bf_lo(unsigned int u) { return __uint_as_float(u << 16); }
__device__ __forceinline__ float bf_hi(unsigned int u) { return __uint_as_float(u & 0xffff0000u); }
__device__ __forceinline__ unsigned int pack_bf16(float lo, float hi) {
    f32x2 f = {lo, hi};
    bf16x2 b = __builtin_convertvector(f, bf16x2);
    return __builtin_bit_cast(unsigned int, b);
}
template <int CTRL>
__device__ __forceinline__ float dpp_add(float v) {
    int t = __builtin_amdgcn_update_dpp(0, __float_as_int(v), CTRL, 0xf, 0xf, true);
    return v + __int_as_float(t);
}
__device__ __forceinline__ float dot2bf(unsigned int a, float c) {
    bf16x2 v = __builtin_bit_cast(bf16x2, a);
    return __builtin_amdgcn_fdot2_f32_bf16(v, v, c, false);
}
#define PIN(x) asm volatile("" : "+v"(x))

extern "C" __global__ __launch_bounds__(TPB) void rmsnorm448_kernel(const void* __restrict__ x, const void* __restrict__ g,
                                                                    void* __restrict__ out, int nbytes) {
    __shared__ u32x4 gsh[64];
    const int lane = threadIdx.x & 63;
    auto xs = __builtin_amdgcn_make_buffer_rsrc((void*)x, 0, nbytes, 0x00020000);
    auto gs = __builtin_amdgcn_make_buffer_rsrc((void*)g, 0, D * 2, 0x00020000);
    auto os = __builtin_amdgcn_make_buffer_rsrc(out, 0, nbytes, 0x00020000);
    const int off0 = ((blockIdx.x * (TPB / 64) + (threadIdx.x >> 6)) * NB * 8 + (lane >> 3)) * (D * 2) + (threadIdx.x & 7) * 16;
    const bool gw = __builtin_amdgcn_readfirstlane(threadIdx.x) == 0;
    u32x4 gl;
    if (gw) gl = __builtin_amdgcn_raw_buffer_load_b128(gs, lane * 16, 0, 0);
    u32x4 v[NB][CH];
#pragma unroll
    for (int b = 0; b < NB; ++b)
#pragma unroll
        for (int k = 0; k < CH; ++k) v[b][k] = __builtin_amdgcn_raw_buffer_load_b128(xs, off0 + b * (8 * D * 2) + k * 128, 0, NT);
    __builtin_amdgcn_sched_barrier(0);
    if (gw) gsh[lane] = gl;
    __syncthreads();
    u32x4 gv[CH];
#pragma unroll
    for (int k = 0; k < CH; ++k) gv[k] = gsh[k * 8 + (lane & 7)];
    f32x2 gf[CH][4];
#pragma unroll
    for (int k = 0; k < CH; ++k)
#pragma unroll
        for (int j = 0; j < 4; ++j) { gf[k][j] = f32x2{bf_lo(gv[k][j]), bf_hi(gv[k][j])}; PIN(gf[k][j]); }
    __builtin_amdgcn_sched_barrier(0);
#pragma unroll
    for (int b = 0; b < NB; ++b) {
        const int off = off0 + b * (8 * D * 2);
        float ss0 = 0.f, ss1 = 0.f;
        f32x2 xg[CH][4];
#pragma unroll
        for (int k = 0; k < CH - 1; ++k) {
#pragma unroll
            for (int j = 0; j < 4; ++j) { if (j & 1) ss1 = dot2bf(v[b][k][j], ss1); else ss0 = dot2bf(v[b][k][j], ss0); }
            PIN(ss0); PIN(ss1);
#pragma unroll
            for (int j = 0; j < 4; ++j) {
                f32x2 a = {bf_lo(v[b][k][j]), bf_hi(v[b][k][j])};
                xg[k][j] = a * gf[k][j];
                PIN(xg[k][j]);
            }
            __builtin_amdgcn_sched_barrier(0);
        }
#pragma unroll
        for (int j = 0; j < 4; ++j) { if (j & 1) ss1 = dot2bf(v[b][CH - 1][j], ss1); else ss0 = dot2bf(v[b][CH - 1][j], ss0); }
        float ss = ss0 + ss1;
        ss = dpp_add<0xB1>(ss);
        ss = dpp_add<0x4E>(ss);
        ss = dpp_add<0x141>(ss);
        const float inv = __builtin_amdgcn_rsqf(ss * (1.0f / D) + 1e-6f);
#pragma unroll
        for (int j = 0; j < 4; ++j) {
            f32x2 a = {bf_lo(v[b][CH - 1][j]), bf_hi(v[b][CH - 1][j])};
            xg[CH - 1][j] = a * gf[CH - 1][j];
        }
        __builtin_amdgcn_sched_barrier(0);
#pragma unroll
        for (int k = 0; k < CH; ++k) {
            u32x4 o;
#pragma unroll
            for (int j = 0; j < 4; ++j) {
                f32x2 r = xg[k][j] * inv;
                o[j] = pack_bf16(r.x, r.y);
            }
            __builtin_amdgcn_raw_buffer_store_b128(o, os, off + k * 128, 0, NT);
        }
        __builtin_amdgcn_sched_barrier(0);
    }
}

extern "C" void kernel_launch(void* const* d_in, const int* in_sizes, int n_in,
                              void* d_out, int out_size, void* d_ws, size_t ws_size,
                              hipStream_t stream) {
    const int rows = in_sizes[0] / D;
    const int rpb = TPB / 8 * NB;
    const int grid = (rows + rpb - 1) / rpb;
    rmsnorm448_kernel<<<grid, TPB, 0, stream>>>(d_in[0], d_in[1], d_out, rows * D * 2);
}
```

```cpp
#include <hip/hip_runtime.h>
#include <stdint.h>

typedef unsigned int u32x4 __attribute__((ext_vector_type(4)));
typedef float f32x2 __attribute__((ext_vector_type(2)));
typedef __bf16 bf16x2 __attribute__((ext_vector_type(2)));

constexpr int D = 448;
constexpr int CH = 7;
constexpr int TPB = 512;
constexpr int NT = 2;

__device__ __forceinline__ float bf_lo(unsigned int u) { return __uint_as_float(u << 16); }
__device__ __forceinline__ float bf_hi(unsigned int u) { return __uint_as_float(u & 0xffff0000u); }
__device__ __forceinline__ unsigned int pack_bf16(float lo, float hi) {
    f32x2 f = {lo, hi};
    bf16x2 b = __builtin_convertvector(f, bf16x2);
    return __builtin_bit_cast(unsigned int, b);
}
template <int CTRL>
__device__ __forceinline__ float dpp_add(float v) {
    int t = __builtin_amdgcn_update_dpp(0, __float_as_int(v), CTRL, 0xf, 0xf, true);
    return v + __int_as_float(t);
}
__device__ __forceinline__ float dot2bf(unsigned int a, float c) {
    bf16x2 v = __builtin_bit_cast(bf16x2, a);
    return __builtin_amdgcn_fdot2_f32_bf16(v, v, c, false);
}
#define PIN(x) asm volatile("" : "+v"(x))

extern "C" __global__ __launch_bounds__(TPB) void rmsnorm448_kernel(const void* __restrict__ x, const void* __restrict__ g,
                                                                    void* __restrict__ out, int nbytes) {
    __shared__ u32x4 gsh[5376];
    const int lane = threadIdx.x & 63;
    auto xs = __builtin_amdgcn_make_buffer_rsrc((void*)x, 0, nbytes, 0x00020000);
    auto gs = __builtin_amdgcn_make_buffer_rsrc((void*)g, 0, D * 2, 0x00020000);
    auto os = __builtin_amdgcn_make_buffer_rsrc(out, 0, nbytes, 0x00020000);
    const int off = (blockIdx.x * (TPB / 8) + (threadIdx.x >> 3)) * (D * 2) + (threadIdx.x & 7) * 16;
    const bool gw = __builtin_amdgcn_readfirstlane(threadIdx.x) == 0;
    u32x4 gl;
    if (gw) gl = __builtin_amdgcn_raw_buffer_load_b128(gs, lane * 16, 0, 0);
    u32x4 v[CH];
#pragma unroll
    for (int k = 0; k < CH; ++k) v[k] = __builtin_amdgcn_raw_buffer_load_b128(xs, off + k * 128, 0, NT);
    __builtin_amdgcn_sched_barrier(0);
    if (gw) gsh[lane] = gl;
    __syncthreads();
    u32x4 gv[CH];
#pragma unroll
    for (int k = 0; k < CH; ++k) gv[k] = gsh[k * 8 + (lane & 7)];
    f32x2 gf[CH][4];
#pragma unroll
    for (int k = 0; k < CH; ++k)
#pragma unroll
        for (int j = 0; j < 4; ++j) { gf[k][j] = f32x2{bf_lo(gv[k][j]), bf_hi(gv[k][j])}; PIN(gf[k][j]); }
    __builtin_amdgcn_sched_barrier(0);

    float ss0 = 0.f, ss1 = 0.f;
    f32x2 xg[CH][4];
#pragma unroll
    for (int k = 0; k < CH - 1; ++k) {
#pragma unroll
        for (int j = 0; j < 4; ++j) { if (j & 1) ss1 = dot2bf(v[k][j], ss1); else ss0 = dot2bf(v[k][j], ss0); }
        PIN(ss0); PIN(ss1);
#pragma unroll
        for (int j = 0; j < 4; ++j) {
            f32x2 a = {bf_lo(v[k][j]), bf_hi(v[k][j])};
            xg[k][j] = a * gf[k][j];
            PIN(xg[k][j]);
        }
        __builtin_amdgcn_sched_barrier(0);
    }
#pragma unroll
    for (int j = 0; j < 4; ++j) { if (j & 1) ss1 = dot2bf(v[CH - 1][j], ss1); else ss0 = dot2bf(v[CH - 1][j], ss0); }
    float ss = ss0 + ss1;
    ss = dpp_add<0xB1>(ss);
    ss = dpp_add<0x4E>(ss);
    ss = dpp_add<0x141>(ss);
    const float inv = __builtin_amdgcn_rsqf(ss * (1.0f / D) + 1e-6f);
#pragma unroll
    for (int j = 0; j < 4; ++j) {
        f32x2 a = {bf_lo(v[CH - 1][j]), bf_hi(v[CH - 1][j])};
        xg[CH - 1][j] = a * gf[CH - 1][j];
    }
    __builtin_amdgcn_sched_barrier(0);
#pragma unroll
    for (int k = 0; k < CH; ++k) {
        u32x4 o;
#pragma unroll
        for (int j = 0; j < 4; ++j) {
            f32x2 r = xg[k][j] * inv;
            o[j] = pack_bf16(r.x, r.y);
        }
        __builtin_amdgcn_raw_buffer_store_b128(o, os, off + k * 128, 0, NT);
    }
}

extern "C" void kernel_launch(void* const* d_in, const int* in_sizes, int n_in,
                              void* d_out, int out_size, void* d_ws, size_t ws_size,
                              hipStream_t stream) {
    const int rows = in_sizes[0] / D;
    const int grid = (rows + TPB / 8 - 1) / (TPB / 8);
    rmsnorm448_kernel<<<grid, TPB, 0, stream>>>(d_in[0], d_in[1], d_out, rows * D * 2);
}
```

```cpp
#include <hip/hip_runtime.h>
#include <stdint.h>

typedef unsigned int u32x4 __attribute__((ext_vector_type(4)));
typedef float f32x2 __attribute__((ext_vector_type(2)));
typedef __bf16 bf16x2 __attribute__((ext_vector_type(2)));

constexpr int D = 448;
constexpr int CH = 7;
constexpr int TPB = 512;
constexpr int NT = 2;

__device__ __forceinline__ float bf_lo(unsigned int u) { return __uint_as_float(u << 16); }
__device__ __forceinline__ float bf_hi(unsigned int u) { return __uint_as_float(u & 0xffff0000u); }
__device__ __forceinline__ unsigned int pack_bf16(float lo, float hi) {
    f32x2 f = {lo, hi};
    bf16x2 b = __builtin_convertvector(f, bf16x2);
    return __builtin_bit_cast(unsigned int, b);
}
template <int CTRL>
__device__ __forceinline__ float dpp_add(float v) {
    int t = __builtin_amdgcn_update_dpp(0, __float_as_int(v), CTRL, 0xf, 0xf, true);
    return v + __int_as_float(t);
}
__device__ __forceinline__ float dot2bf(unsigned int a, float c) {
    bf16x2 v = __builtin_bit_cast(bf16x2, a);
    return __builtin_amdgcn_fdot2_f32_bf16(v, v, c, false);
}
#define PIN(x) asm volatile("" : "+v"(x))

extern "C" __global__ __launch_bounds__(TPB) void rmsnorm448_kernel(const void* __restrict__ x, const void* __restrict__ g,
                                                                    void* __restrict__ out, int nbytes) {
    __shared__ u32x4 gsh[64];
    const int lane = threadIdx.x & 63;
    int off = (blockIdx.x * (TPB / 8) + (threadIdx.x >> 3)) * (D * 2) + (threadIdx.x & 7) * 16;
    PIN(off);
    asm volatile("" ::"s"(x), "s"(g), "s"(out), "s"(nbytes));
    auto xs = __builtin_amdgcn_make_buffer_rsrc((void*)x, 0, nbytes, 0x00020000);
    auto gs = __builtin_amdgcn_make_buffer_rsrc((void*)g, 0, D * 2, 0x00020000);
    auto os = __builtin_amdgcn_make_buffer_rsrc(out, 0, nbytes, 0x00020000);
    const bool gw = __builtin_amdgcn_readfirstlane(threadIdx.x) == 0;
    u32x4 gl;
    if (gw) gl = __builtin_amdgcn_raw_buffer_load_b128(gs, lane * 16, 0, 0);
    u32x4 v[CH];
#pragma unroll
    for (int k = 0; k < CH; ++k) v[k] = __builtin_amdgcn_raw_buffer_load_b128(xs, off + k * 128, 0, NT);
    __builtin_amdgcn_sched_barrier(0);
    if (gw) gsh[lane] = gl;
    __syncthreads();
    u32x4 gv[CH];
#pragma unroll
    for (int k = 0; k < CH; ++k) gv[k] = gsh[k * 8 + (lane & 7)];
    f32x2 gf[CH][4];
#pragma unroll
    for (int k = 0; k < CH; ++k)
#pragma unroll
        for (int j = 0; j < 4; ++j) { gf[k][j] = f32x2{bf_lo(gv[k][j]), bf_hi(gv[k][j])}; PIN(gf[k][j]); }
    __builtin_amdgcn_sched_barrier(0);

    float ss0 = 0.f, ss1 = 0.f;
    f32x2 xg[CH][4];
#pragma unroll
    for (int k = 0; k < CH - 1; ++k) {
#pragma unroll
        for (int j = 0; j < 4; ++j) { if (j & 1) ss1 = dot2bf(v[k][j], ss1); else ss0 = dot2bf(v[k][j], ss0); }
        PIN(ss0); PIN(ss1);
#pragma unroll
        for (int j = 0; j < 4; ++j) {
            f32x2 a = {bf_lo(v[k][j]), bf_hi(v[k][j])};
            xg[k][j] = a * gf[k][j];
            PIN(xg[k][j]);
        }
        __builtin_amdgcn_sched_barrier(0);
    }
#pragma unroll
    for (int j = 0; j < 4; ++j) { if (j & 1) ss1 = dot2bf(v[CH - 1][j], ss1); else ss0 = dot2bf(v[CH - 1][j], ss0); }
    float ss = ss0 + ss1;
    ss = dpp_add<0xB1>(ss);
    ss = dpp_add<0x4E>(ss);
    ss = dpp_add<0x141>(ss);
    const float inv = __builtin_amdgcn_rsqf(ss * (1.0f / D) + 1e-6f);
#pragma unroll
    for (int j = 0; j < 4; ++j) {
        f32x2 a = {bf_lo(v[CH - 1][j]), bf_hi(v[CH - 1][j])};
        xg[CH - 1][j] = a * gf[CH - 1][j];
    }
    __builtin_amdgcn_sched_barrier(0);
    int soff = off;
    PIN(soff);
#pragma unroll
    for (int k = 0; k < CH; ++k) {
        u32x4 o;
#pragma unroll
        for (int j = 0; j < 4; ++j) {
            f32x2 r = xg[k][j] * inv;
            o[j] = pack_bf16(r.x, r.y);
        }
        __builtin_amdgcn_raw_buffer_store_b128(o, os, soff + k * 128, 0, NT);
    }
}

extern "C" void kernel_launch(void* const* d_in, const int* in_sizes, int n_in,
                              void* d_out, int out_size, void* d_ws, size_t ws_size,
                              hipStream_t stream) {
    const int rows = in_sizes[0] / D;
    const int grid = (rows + TPB / 8 - 1) / (TPB / 8);
    rmsnorm448_kernel<<<grid, TPB, 0, stream>>>(d_in[0], d_in[1], d_out, rows * D * 2);
}
```

```cpp
#include <hip/hip_runtime.h>
#include <hip/hip_bf16.h>
#include <stdint.h>

#define LD_AUX @LD_AUX@
#define ST_AUX @ST_AUX@
#define TPB @TPB@
#define QSEL @QSEL@
#define KMUL @KMUL@

typedef unsigned int u32x4 __attribute__((ext_vector_type(4)));
typedef float f32x2 __attribute__((ext_vector_type(2)));
typedef __bf16 bf16x2 __attribute__((ext_vector_type(2)));
constexpr int D = 448;
constexpr int CH = 7;
__device__ __forceinline__ float bf_lo(unsigned int u) { return __uint_as_float(u << 16); }
__device__ __forceinline__ float bf_hi(unsigned int u) { return __uint_as_float(u & 0xffff0000u); }
__device__ __forceinline__ unsigned int pack_bf16(float lo, float hi) {
    f32x2 f = {lo, hi};
    bf16x2 b = __builtin_convertvector(f, bf16x2);
    return __builtin_bit_cast(unsigned int, b);
}
template <int CTRL>
__device__ __forceinline__ float dpp_add(float v) {
    int t = __builtin_amdgcn_update_dpp(0, __float_as_int(v), CTRL, 0xf, 0xf, true);
    return v + __int_as_float(t);
}
__device__ __forceinline__ unsigned long long stamp() {
    unsigned long long t;
    __builtin_amdgcn_sched_barrier(0);
    asm volatile("s_memrealtime %0\n\ts_waitcnt lgkmcnt(0)" : "=s"(t) :: "memory");
    __builtin_amdgcn_sched_barrier(0);
    return t;
}
constexpr int NS = 8;
__global__ __launch_bounds__(TPB) void tl_kernel(const void* __restrict__ x, const void* __restrict__ g,
                                                 void* __restrict__ out, int nbytes, unsigned long long* __restrict__ dbg) {
    unsigned long long t0 = stamp();
    const int tid = blockIdx.x * TPB + threadIdx.x;
    const int row = tid >> 3;
    const int sub = tid & 7;
    auto xs = __builtin_amdgcn_make_buffer_rsrc((void*)x, 0, nbytes, 0x00020000);
    auto gs = __builtin_amdgcn_make_buffer_rsrc((void*)g, 0, D * 2, 0x00020000);
    auto os = __builtin_amdgcn_make_buffer_rsrc(out, 0, nbytes, 0x00020000);
    const int off = row * (D * 2) + sub * 16;
    asm volatile("" :: "s"(x), "s"(out), "s"(g));
    unsigned long long t1 = stamp();
    u32x4 v[CH], gv[CH];
#pragma unroll
    for (int k = 0; k < CH; ++k) v[k] = __builtin_amdgcn_raw_buffer_load_b128(xs, off + k * 128, 0, LD_AUX);
#pragma unroll
    for (int k = 0; k < CH; ++k) gv[k] = __builtin_amdgcn_raw_buffer_load_b128(gs, sub * 16 + k * 128, 0, 0);
    unsigned long long t2 = stamp();
    asm volatile("s_waitcnt vmcnt(13)" : "+v"(v[0]) :: "memory");
    unsigned long long t3 = stamp();
    asm volatile("s_waitcnt vmcnt(0)" : "+v"(v[0]), "+v"(v[1]), "+v"(v[2]), "+v"(v[3]), "+v"(v[4]), "+v"(v[5]), "+v"(v[6]) :: "memory");
    unsigned long long t4 = stamp();
    float ss0 = 0.f, ss1 = 0.f;
#pragma unroll
    for (int k = 0; k < CH; ++k) {
#pragma unroll
        for (int j = 0; j < 4; ++j) {
            float a = bf_lo(v[k][j]), b = bf_hi(v[k][j]);
            ss0 = fmaf(a, a, ss0);
            ss1 = fmaf(b, b, ss1);
        }
    }
    float ss = ss0 + ss1;
    ss = dpp_add<0xB1>(ss);
    ss = dpp_add<0x4E>(ss);
    ss = dpp_add<0x141>(ss);
    const float inv = __builtin_amdgcn_rsqf(ss * (1.0f / D) + 1e-6f);
#pragma unroll
    for (int k = 0; k < CH; ++k) {
        u32x4 o;
#pragma unroll
        for (int j = 0; j < 4; ++j) {
            float a = bf_lo(v[k][j]) * inv * bf_lo(gv[k][j]);
            float b = bf_hi(v[k][j]) * inv * bf_hi(gv[k][j]);
            o[j] = pack_bf16(a, b);
        }
        __builtin_amdgcn_raw_buffer_store_b128(o, os, off + k * 128, 0, ST_AUX);
    }
    unsigned long long t5 = stamp();
    asm volatile("s_waitcnt vmcnt(0)" ::: "memory");
    unsigned long long t6 = stamp();
    if ((threadIdx.x & 63) == 0) {
        unsigned long long* p = dbg + (size_t)(tid >> 6) * NS;
        p[0] = t0; p[1] = t1; p[2] = t2; p[3] = t3; p[4] = t4; p[5] = t5; p[6] = t6; p[7] = (unsigned)__builtin_amdgcn_s_getreg(20 | (3 << 11)) & 0xfu;
    }
}

__global__ __launch_bounds__(1024) void spin_kernel(const unsigned long long* __restrict__ dbg, int nw, int qsel, int kmul) {
    unsigned long long tent = stamp();
    __shared__ unsigned long long smin[16], smax[16], ssum[16];
    unsigned long long mn = ~0ull, mx = 0, sm = 0;
    const int s = qsel == 0 ? 0 : (qsel <= 7 ? qsel - 1 : (qsel <= 17 ? qsel - 11 : 6));
    for (int w = threadIdx.x; w < nw; w += 1024) {
        unsigned long long a = dbg[w * NS], b = dbg[w * NS + s];
        unsigned long long xc = dbg[w * NS + 7];
        mn = a < mn ? a : mn;
        if (qsel >= 30 && qsel < 38) { if (xc == (unsigned long long)(qsel - 30)) mx = (~a) > mx ? (~a) : mx; }
        else if (qsel >= 40 && qsel < 48) { if (xc == (unsigned long long)(qsel - 40)) mx = b > mx ? b : mx; }
        else if (qsel == 60) { unsigned long long f = dbg[(w & ~7) * NS]; unsigned long long dd = a > f ? a - f : f - a; mx = dd > mx ? dd : mx; }
        else mx = b > mx ? b : mx;
        sm += (b & 0xffffffffffull);
    }
    for (int d = 32; d > 0; d >>= 1) {
        unsigned long long a = __shfl_xor(mn, d, 64), b = __shfl_xor(mx, d, 64), c = __shfl_xor(sm, d, 64);
        mn = a < mn ? a : mn; mx = b > mx ? b : mx; sm += c;
    }
    if ((threadIdx.x & 63) == 0) { smin[threadIdx.x >> 6] = mn; smax[threadIdx.x >> 6] = mx; ssum[threadIdx.x >> 6] = sm; }
    __syncthreads();
    if (threadIdx.x == 0) {
        sm = 0;
        for (int i = 0; i < 16; ++i) { mn = smin[i] < mn ? smin[i] : mn; mx = smax[i] > mx ? smax[i] : mx; sm += ssum[i]; }
        unsigned long long now = stamp();
        unsigned long long q = 0;
        if (qsel >= 1 && qsel <= 7) q = mx - mn;
        else if (qsel >= 11 && qsel <= 17) q = sm / nw - (mn & 0xffffffffffull);
        else if (qsel >= 30 && qsel < 38) q = (~mx) - mn;
        else if (qsel >= 40 && qsel < 48) q = mx - mn;
        else if (qsel == 60) q = mx;
        else if (qsel == 50) q = 20 * (dbg[7] + 8 * ((dbg[4 * NS + 7] - dbg[7]) & 7)) + 10;
        else if (qsel == 20) q = tent - mx;
        else if (qsel == 21) q = now - mn;
        else if (qsel == 22) q = now - tent;
        if (q > 3000) q = 3000;
        now = stamp();
        unsigned long long until = now + q * kmul;
        while (__builtin_amdgcn_s_memrealtime() < until) __builtin_amdgcn_s_sleep(1);
    }
}

extern "C" void kernel_launch(void* const* d_in, const int* in_sizes, int n_in,
                              void* d_out, int out_size, void* d_ws, size_t ws_size,
                              hipStream_t stream) {
    int rows = in_sizes[0] / D;
    int threads = rows * 8;
    int grid = (threads + TPB - 1) / TPB;
    tl_kernel<<<grid, TPB, 0, stream>>>(d_in[0], d_in[1], d_out, rows * D * 2, (unsigned long long*)d_ws);
    spin_kernel<<<1, 1024, 0, stream>>>((const unsigned long long*)d_ws, rows / 8, QSEL, KMUL);
}
```

```cpp
#include <hip/hip_runtime.h>
#include <stdint.h>

typedef unsigned int u32x4 __attribute__((ext_vector_type(4)));
typedef float f32x2 __attribute__((ext_vector_type(2)));
typedef __bf16 bf16x2 __attribute__((ext_vector_type(2)));

constexpr int D = 448;
constexpr int CH = 7;
constexpr int TPB = 512;
constexpr int NT = 2;

__device__ __forceinline__ float bf_lo(unsigned int u) { return __uint_as_float(u << 16); }
__device__ __forceinline__ float bf_hi(unsigned int u) { return __uint_as_float(u & 0xffff0000u); }
__device__ __forceinline__ unsigned int pack_bf16(float lo, float hi) {
    f32x2 f = {lo, hi};
    bf16x2 b = __builtin_convertvector(f, bf16x2);
    return __builtin_bit_cast(unsigned int, b);
}
template <int CTRL>
__device__ __forceinline__ float dpp_add(float v) {
    int t = __builtin_amdgcn_update_dpp(0, __float_as_int(v), CTRL, 0xf, 0xf, true);
    return v + __int_as_float(t);
}
__device__ __forceinline__ float dot2bf(unsigned int a, float c) {
    bf16x2 v = __builtin_bit_cast(bf16x2, a);
    return __builtin_amdgcn_fdot2_f32_bf16(v, v, c, false);
}
#define PIN(x) asm volatile("" : "+v"(x))

extern "C" __global__ __launch_bounds__(TPB) void rmsnorm448_kernel(const void* __restrict__ x, const void* __restrict__ g,
                                                                    void* __restrict__ out, int nbytes) {
    __shared__ u32x4 gsh[64];
    const int lane = threadIdx.x & 63;
    int off = (blockIdx.x * (TPB / 8) + (threadIdx.x >> 3)) * (D * 2) + (threadIdx.x & 7) * 16;
    PIN(off);
    asm volatile("" ::"s"(x), "s"(g), "s"(out), "s"(nbytes));
    auto xs = __builtin_amdgcn_make_buffer_rsrc((void*)x, 0, nbytes, 0x00020000);
    auto gs = __builtin_amdgcn_make_buffer_rsrc((void*)g, 0, D * 2, 0x00020000);
    auto os = __builtin_amdgcn_make_buffer_rsrc(out, 0, nbytes, 0x00020000);
    const bool gw = __builtin_amdgcn_readfirstlane(threadIdx.x) == 0;
    u32x4 gl;
    if (gw) gl = __builtin_amdgcn_raw_buffer_load_b128(gs, lane * 16, 0, 0);
    u32x4 v[CH];
#pragma unroll
    for (int k = 0; k < CH; ++k) v[k] = __builtin_amdgcn_raw_buffer_load_b128(xs, off + k * 128, 0, NT);
    __builtin_amdgcn_sched_barrier(0);
    if (gw) gsh[lane] = gl;
    __syncthreads();
    u32x4 gv[CH];
#pragma unroll
    for (int k = 0; k < CH; ++k) gv[k] = gsh[k * 8 + (lane & 7)];
    f32x2 gf[CH][4];
#pragma unroll
    for (int k = 0; k < CH; ++k)
#pragma unroll
        for (int j = 0; j < 4; ++j) { gf[k][j] = f32x2{bf_lo(gv[k][j]), bf_hi(gv[k][j])}; PIN(gf[k][j]); }
    __builtin_amdgcn_sched_barrier(0);

    float ss0 = 0.f, ss1 = 0.f;
    f32x2 xg[CH][4];
#pragma unroll
    for (int k = 0; k < CH - 1; ++k) {
#pragma unroll
        for (int j = 0; j < 4; ++j) { if (j & 1) ss1 = dot2bf(v[k][j], ss1); else ss0 = dot2bf(v[k][j], ss0); }
        PIN(ss0); PIN(ss1);
#pragma unroll
        for (int j = 0; j < 4; ++j) {
            f32x2 a = {bf_lo(v[k][j]), bf_hi(v[k][j])};
            xg[k][j] = a * gf[k][j];
            PIN(xg[k][j]);
        }
        __builtin_amdgcn_sched_barrier(0);
    }
#pragma unroll
    for (int j = 0; j < 4; ++j) { if (j & 1) ss1 = dot2bf(v[CH - 1][j], ss1); else ss0 = dot2bf(v[CH - 1][j], ss0); }
    float ss = ss0 + ss1;
    ss = dpp_add<0xB1>(ss);
    ss = dpp_add<0x4E>(ss);
    ss = dpp_add<0x141>(ss);
    const float inv = __builtin_amdgcn_rsqf(ss * (1.0f / D) + 1e-6f);
#pragma unroll
    for (int j = 0; j < 4; ++j) {
        f32x2 a = {bf_lo(v[CH - 1][j]), bf_hi(v[CH - 1][j])};
        xg[CH - 1][j] = a * gf[CH - 1][j];
    }
    __builtin_amdgcn_sched_barrier(0);
    int soff = off;
    PIN(soff);
#pragma unroll
    for (int k = 0; k < CH; ++k) {
        u32x4 o;
#pragma unroll
        for (int j = 0; j < 4; ++j) {
            f32x2 r = xg[k][j] * inv;
            o[j] = pack_bf16(r.x, r.y);
        }
        __builtin_amdgcn_raw_buffer_store_b128(o, os, soff + k * 128, 0, NT);
    }
}

extern "C" void kernel_launch(void* const* d_in, const int* in_sizes, int n_in,
                              void* d_out, int out_size, void* d_ws, size_t ws_size,
                              hipStream_t stream) {
    (void)n_in; (void)out_size; (void)d_ws; (void)ws_size;
    const int rows = in_sizes[0] / D;
    const int grid = (rows + TPB / 8 - 1) / (TPB / 8);
    rmsnorm448_kernel<<<grid, TPB, 0, stream>>>(d_in[0], d_in[1], d_out, rows * D * 2);
}
```
